```python
import torch

def _softmax(x, axis=-1):
    return torch.softmax(x, dim=axis)
def _gelu(x):
    return torch.nn.functional.gelu(x, approximate="tanh")
def _silu(x):
    return torch.nn.functional.silu(x)
def _sigmoid(x):
    return torch.sigmoid(x)
def _layernorm(x, axis=-1, eps=1e-5):
    m = x.mean(dim=axis, keepdim=True)
    v = x.var(dim=axis, keepdim=True, unbiased=False)
    return (x - m) / torch.sqrt(v + eps)
def _scatter_add(tgt, idx, src, axis=0):
    return tgt.clone().index_add_(axis, idx, src)
def _segment_sum(src, seg, n):
    out = torch.zeros((n,) + src.shape[1:], dtype=src.dtype)
    return out.index_add_(0, seg, src)
def _pad(x, pw):
    flat = tuple(v for pair in reversed(pw) for v in pair)
    return torch.nn.functional.pad(x, flat)

kernel_name = 'gated_mix__ssm_conv_s4d__linear_kernel_hedgehog__prompt'

def setup_inputs():

    _w = (lambda a: (lambda t: t.to(torch.bfloat16) if t.is_floating_point() else t)(torch.as_tensor(a)))
    def _A_setup():
        Bsz, L, D, N = 1, 2048, 512, 128
        return {
            "u": _w((lambda *s: torch.randn(*s, dtype=torch.bfloat16) * 0.05)(Bsz, L, D)),
            "log_A_real": _w((lambda *s: torch.randn(*s, dtype=torch.bfloat16) * 3.2)(D, N) * 0.5),
            "A_imag": _w((lambda *s: torch.randn(*s, dtype=torch.bfloat16) * 0.05)(D, N) * 3.14),
            "C_real": _w((lambda *s: torch.randn(*s, dtype=torch.bfloat16) * 0.4)(D, N) / N ** 0.5),
            "C_imag": _w((lambda *s: torch.randn(*s, dtype=torch.bfloat16) * 0.4)(D, N) / N ** 0.5),
            "log_dt": _w((lambda *s: torch.randn(*s, dtype=torch.bfloat16) * 0.05)(D) * 0.5 - 4.0),
            "D_skip": _w((lambda *s: torch.randn(*s, dtype=torch.bfloat16) * 0.05)(D)),
        }
    _A_in = _A_setup()

    def _B_setup():
        B, S, D, H = 1, 2048, 512, 32
        Dh = D // H
        return {
            "x": _w((lambda *s: torch.randn(*s, dtype=torch.bfloat16) * 0.05)(B, S, D)),
            "wqkv": _w((lambda *s: torch.randn(*s, dtype=torch.bfloat16) * 0.8)(D, 3 * D)),
            "w_phi": _w((lambda *s: torch.randn(*s, dtype=torch.bfloat16) * 0.8)(H, Dh, Dh) * (Dh ** -0.5)),
            "wo": _w((lambda *s: torch.randn(*s, dtype=torch.bfloat16) * 0.05)(D, D)),
        }
    _B_in = _B_setup()

    return {
        "A_u": _A_in["u"],
        "A_log_A_real": _A_in["log_A_real"],
        "A_A_imag": _A_in["A_imag"],
        "A_C_real": _A_in["C_real"],
        "A_C_imag": _A_in["C_imag"],
        "A_log_dt": _A_in["log_dt"],
        "A_D_skip": _A_in["D_skip"],
        "B_x": _B_in["x"],
        "B_wqkv": _B_in["wqkv"],
        "B_w_phi": _B_in["w_phi"],
        "B_wo": _B_in["wo"],
        "gate_w": _w((lambda *s: torch.randn(*s, dtype=torch.bfloat16) * 0.05)(512) * 16.0),
    }

def reference(A_u, A_log_A_real, A_A_imag, A_C_real, A_C_imag, A_log_dt, A_D_skip, B_x, B_wqkv, B_w_phi, B_wo, gate_w):
    def _A_ref(u, log_A_real, A_imag, C_real, C_imag, log_dt, D_skip):
        L = u.shape[1]
        dt = torch.exp(log_dt)
        A = -torch.exp(log_A_real) + 1j * A_imag
        C = (C_real + 1j * C_imag) * dt[:, None]
        pos = (lambda x, dt: x.to(dt))(torch.arange(L), log_dt.dtype)
        P = torch.exp((A * dt[:, None])[:, :, None] * pos[None, None, :])
        K = 2.0 * torch.einsum("dn,dnl->dl", C, P).real
        L2 = 2 * L
        u_t = (lambda x, *d: x.permute(*d))(u, 0, 2, 1)
        Kf = (lambda x, *a, **k: torch.fft.rfft(x.float(), *a, **k))(K, L2)
        Uf = (lambda x, *a, **k: torch.fft.rfft(x.float(), *a, **k))(u_t, L2)
        y = (lambda x, *a, **k: torch.fft.irfft(x, *a, **k).to(torch.bfloat16))(Uf * Kf[None, :, :], L2)[:, :, :L]
        y = (lambda x, *d: x.permute(*d))(y, 0, 2, 1)
        return y + u * D_skip[None, None, :]

    def _B_ref(x, wqkv, w_phi, wo):
        B, S, D = x.shape
        H = 32; Dh = D // H
        qkv = (lambda x, *d: x.permute(*d))((x @ wqkv).reshape(B, S, 3, H, Dh), 2, 0, 3, 1, 4)
        q, k, v = qkv[0], qkv[1], qkv[2]
        pq = torch.einsum("bhsd,hdf->bhsf", q, w_phi)
        pk = torch.einsum("bhsd,hdf->bhsf", k, w_phi)
        phi_q = torch.concatenate([_softmax(pq, axis=-1), _softmax(-pq, axis=-1)], axis=-1)
        phi_k = torch.concatenate([_softmax(pk, axis=-1), _softmax(-pk, axis=-1)], axis=-1)
        kv = torch.einsum("bhsf,bhsd->bhfd", phi_k, v)
        z  = torch.einsum("bhsf->bhf", phi_k)
        num = torch.einsum("bhqf,bhfd->bhqd", phi_q, kv)
        den = torch.einsum("bhqf,bhf->bhq", phi_q, z)[..., None] + 1e-6
        o = num / den
        return (lambda x, *d: x.permute(*d))(o, 0, 2, 1, 3).reshape(B, S, D) @ wo

    x = A_u
    g = _sigmoid((x * gate_w).sum(-1, keepdims=True))
    a = _A_ref(x, A_log_A_real, A_A_imag, A_C_real, A_C_imag, A_log_dt, A_D_skip)
    b = _B_ref(x, B_wqkv, B_w_phi, B_wo)
    return g * a + (1 - g) * b
```

```cpp
#include <hip/hip_runtime.h>
#include <hip/hip_bf16.h>
#include <stdint.h>

typedef unsigned short u16;
static __device__ __forceinline__ float bf2f(u16 v) { return __uint_as_float(((unsigned)v) << 16); }
static __device__ __forceinline__ u16 f2bf(float f) {
    unsigned u = __float_as_uint(f);
    u += 0x7FFF + ((u >> 16) & 1);
    return (u16)(u >> 16);
}
static __device__ __forceinline__ float rbf(float f) { return bf2f(f2bf(f)); }

constexpr int L = 2048, D = 512, N = 128, H = 32;

__global__ void n_kgen(const u16* logA, const u16* Aim, const u16* Cre, const u16* Cim, const u16* logdt, float* K) {
    int d = blockIdx.y;
    int l = blockIdx.x * 256 + threadIdx.x;
    float dt = rbf(expf(bf2f(logdt[d])));
    float pos = rbf((float)l);
    float acc = 0.f;
    for (int n = 0; n < N; ++n) {
        float a = -rbf(expf(bf2f(logA[d * N + n]))) * dt;
        float b = bf2f(Aim[d * N + n]) * dt;
        float cr = bf2f(Cre[d * N + n]) * dt, ci = bf2f(Cim[d * N + n]) * dt;
        float e = expf(a * pos);
        float s, c;
        sincosf(b * pos, &s, &c);
        acc += cr * (e * c) - ci * (e * s);
    }
    K[d * L + l] = 2.f * acc;
}

__global__ void n_conv(const u16* u, const float* K, const u16* Dskip, u16* a_out) {
    int t = blockIdx.x;
    int d = blockIdx.y * 256 + threadIdx.x;
    float acc = 0.f;
    for (int j = 0; j <= t; ++j) acc += K[d * L + j] * bf2f(u[(t - j) * D + d]);
    float y = rbf(acc);
    float s = rbf(bf2f(u[t * D + d]) * bf2f(Dskip[d]));
    a_out[t * D + d] = f2bf(y + s);
}

__global__ void n_gemm(const u16* A, const u16* B, u16* C, int Nn, int Kk) {
    int t = blockIdx.x;
    int c = blockIdx.y * 256 + threadIdx.x;
    float acc = 0.f;
    for (int k = 0; k < Kk; ++k) acc += bf2f(A[t * Kk + k]) * bf2f(B[k * Nn + c]);
    C[t * Nn + c] = f2bf(acc);
}

__global__ void n_phi(const u16* qkv, const u16* wphi, u16* phiq, u16* phik) {
    int t = blockIdx.x;
    int h = threadIdx.x & 31, which = threadIdx.x >> 5;
    float p[16];
    for (int f = 0; f < 16; ++f) {
        float acc = 0.f;
        for (int dd = 0; dd < 16; ++dd) acc += bf2f(qkv[t * 1536 + which * 512 + h * 16 + dd]) * bf2f(wphi[h * 256 + dd * 16 + f]);
        p[f] = rbf(acc);
    }
    float mx = -1e30f, mn = 1e30f;
    for (int f = 0; f < 16; ++f) { mx = fmaxf(mx, p[f]); mn = fminf(mn, p[f]); }
    float sp = 0.f, sn = 0.f;
    for (int f = 0; f < 16; ++f) { sp += expf(p[f] - mx); sn += expf(mn - p[f]); }
    u16* o = (which ? phik : phiq) + (t * H + h) * 32;
    for (int f = 0; f < 16; ++f) { o[f] = f2bf(expf(p[f] - mx) / sp); o[16 + f] = f2bf(expf(mn - p[f]) / sn); }
}

__global__ void n_kv(const u16* qkv, const u16* phik, u16* kv, u16* z) {
    int h = blockIdx.x;
    int f = threadIdx.x >> 4, dd = threadIdx.x & 15;
    float acc = 0.f, zz = 0.f;
    for (int t = 0; t < L; ++t) {
        float p = bf2f(phik[(t * H + h) * 32 + f]);
        acc += p * bf2f(qkv[t * 1536 + 1024 + h * 16 + dd]);
        zz += p;
    }
    kv[(h * 32 + f) * 16 + dd] = f2bf(acc);
    if (dd == 0) z[h * 32 + f] = f2bf(zz);
}

__global__ void n_o(const u16* phiq, const u16* kv, const u16* z, u16* o) {
    int t = blockIdx.x;
    int c = threadIdx.x;
    int h = c >> 4, dd = c & 15;
    float num = 0.f, den = 0.f;
    for (int f = 0; f < 32; ++f) {
        float p = bf2f(phiq[(t * H + h) * 32 + f]);
        num += p * bf2f(kv[(h * 32 + f) * 16 + dd]);
        den += p * bf2f(z[h * 32 + f]);
    }
    float dn = rbf(rbf(den) + 1e-6f);
    o[t * D + c] = f2bf(rbf(num) / dn);
}

__global__ void n_final(const u16* u, const u16* gw, const u16* a, const u16* b, u16* out) {
    __shared__ float red[512];
    int t = blockIdx.x, c = threadIdx.x;
    red[c] = rbf(bf2f(u[t * D + c]) * bf2f(gw[c]));
    __syncthreads();
    for (int s = 256; s > 0; s >>= 1) { if (c < s) red[c] += red[c + s]; __syncthreads(); }
    float sm = rbf(red[0]);
    float g = rbf(1.f / (1.f + expf(-sm)));
    float t1 = rbf(g * bf2f(a[t * D + c]));
    float t2 = rbf(rbf(1.f - g) * bf2f(b[t * D + c]));
    out[t * D + c] = f2bf(t1 + t2);
}

extern "C" void kernel_launch(void* const* d_in, const int* in_sizes, int n_in,
                              void* d_out, int out_size, void* d_ws, size_t ws_size,
                              hipStream_t stream) {
    const u16* u = (const u16*)d_in[0];
    const u16* logA = (const u16*)d_in[1];
    const u16* Aim = (const u16*)d_in[2];
    const u16* Cre = (const u16*)d_in[3];
    const u16* Cim = (const u16*)d_in[4];
    const u16* logdt = (const u16*)d_in[5];
    const u16* Dskip = (const u16*)d_in[6];
    const u16* wqkv = (const u16*)d_in[8];
    const u16* wphi = (const u16*)d_in[9];
    const u16* wo = (const u16*)d_in[10];
    const u16* gw = (const u16*)d_in[11];
    char* ws = (char*)d_ws;
    float* K = (float*)ws;
    u16* a = (u16*)(ws + (4 << 20));
    u16* qkv = (u16*)(ws + (6 << 20));
    u16* phiq = (u16*)(ws + (12 << 20));
    u16* phik = (u16*)(ws + (16 << 20));
    u16* kv = (u16*)(ws + (20 << 20));
    u16* z = (u16*)(ws + (21 << 20));
    u16* o = (u16*)(ws + (22 << 20));
    u16* b = (u16*)(ws + (24 << 20));
    n_kgen<<<dim3(L / 256, D), 256, 0, stream>>>(logA, Aim, Cre, Cim, logdt, K);
    n_conv<<<dim3(L, 2), 256, 0, stream>>>(u, K, Dskip, a);
    n_gemm<<<dim3(L, 6), 256, 0, stream>>>(u, wqkv, qkv, 1536, 512);
    n_phi<<<L, 64, 0, stream>>>(qkv, wphi, phiq, phik);
    n_kv<<<H, 512, 0, stream>>>(qkv, phik, kv, z);
    n_o<<<L, 512, 0, stream>>>(phiq, kv, z, o);
    n_gemm<<<dim3(L, 2), 256, 0, stream>>>(o, wo, b, 512, 512);
    n_final<<<L, 512, 0, stream>>>(u, gw, a, b, (u16*)d_out);
}
```

```cpp
#include <hip/hip_runtime.h>
#include <stdint.h>

#define DI __device__ __forceinline__
#define LAS __attribute__((address_space(3)))
typedef unsigned short u16;
typedef short bf16x8 __attribute__((ext_vector_type(8)));
typedef short s16x4 __attribute__((ext_vector_type(4)));
typedef short v4i16_t __attribute__((ext_vector_type(4)));
typedef float f32x2 __attribute__((ext_vector_type(2)));
typedef float f32x4 __attribute__((ext_vector_type(4)));
typedef float f32x16 __attribute__((ext_vector_type(16)));
typedef unsigned u32x2 __attribute__((ext_vector_type(2)));
typedef unsigned u32x4 __attribute__((ext_vector_type(4)));
typedef __bf16 bf2_t __attribute__((ext_vector_type(2)));
typedef LAS char* lds_t;

constexpr int SEQ = 2048, DM = 512;

constexpr size_t WS_PHIQ = 0;
constexpr size_t WS_YT   = 4u << 20;
constexpr size_t WS_SLAB = 6u << 20;
constexpr size_t WS_ZSL  = WS_SLAB + (2u << 20);
constexpr size_t WS_KVF  = WS_ZSL + (1u << 16) * 2;
constexpr size_t WS_ZF   = WS_KVF + (1u << 15);
constexpr size_t WS_G    = WS_ZF + (1u << 12);
constexpr size_t WS_STAMP = 16u << 20;

#define STAMP(k) do { } while (0)

DI float bf2f(unsigned v) { return __uint_as_float(v << 16); }
DI float bflo(unsigned v) { return __uint_as_float(v << 16); }
DI float bfhi(unsigned v) { return __uint_as_float(v & 0xffff0000u); }
DI unsigned pk(float lo, float hi) { f32x2 v = {lo, hi}; return __builtin_bit_cast(unsigned, __builtin_convertvector(v, bf2_t)); }
DI float rbf(float f) { return bflo(pk(f, f)); }
DI bf16x8 as_frag(u32x4 v) { return __builtin_bit_cast(bf16x8, v); }
DI bf16x8 frag2(u32x2 a, u32x2 b) { u32x4 v = {a.x, a.y, b.x, b.y}; return as_frag(v); }
DI u32x4 lds_r128(lds_t p) { return *(LAS u32x4*)p; }
DI u32x2 lds_r64v(lds_t p) { return *(volatile LAS u32x2*)p; }
DI u32x2 lds_tr(lds_t p) { return __builtin_bit_cast(u32x2, __builtin_amdgcn_ds_read_tr16_b64_v4i16((LAS v4i16_t*)p)); }
DI void lds_w128(lds_t p, u32x4 v) { *(LAS u32x4*)p = v; }
DI void lds_w64(lds_t p, u32x2 v) { *(LAS u32x2*)p = v; }
DI void lds_w32(lds_t p, unsigned v) { *(LAS unsigned*)p = v; }
DI f32x16 mfma32(bf16x8 a, bf16x8 b, f32x16 c) { return __builtin_amdgcn_mfma_f32_32x32x16_bf16(a, b, c, 0, 0, 0); }
DI f32x4 mfma16(bf16x8 a, bf16x8 b, f32x4 c) { return __builtin_amdgcn_mfma_f32_16x16x32_bf16(a, b, c, 0, 0, 0); }
DI unsigned ldsaddr(lds_t p) { return (unsigned)(size_t)p; }

DI void glds16(unsigned voff, const void* sbase, unsigned dst) {
    unsigned keep;
    asm volatile("s_mov_b32 %0, m0\n\ts_mov_b32 m0, %3\n\ts_nop 0\n\tglobal_load_lds_dwordx4 %1, %2\n\ts_mov_b32 m0, %0"
                 : "=&s"(keep) : "v"(voff), "s"(sbase), "s"(dst) : "memory");
}
#define WAIT_BAR(n) asm volatile("s_waitcnt vmcnt(" #n ") lgkmcnt(0)\n\ts_barrier" ::: "memory")
#define LDS_BAR() asm volatile("s_waitcnt lgkmcnt(0)\n\ts_barrier" ::: "memory")

constexpr float LOG2E = 1.4426950408889634f;

constexpr int P1_X = 0, P1_W = 49152;
constexpr int P1_PH = 0, P1_V = 16384;

template <bool STORE_Q>
DI void softmax_pm(const f32x16& p, u32x4& plus, u32x4& minus) {
    float mx = fmaxf(fmaxf(fmaxf(p[0], p[1]), fmaxf(p[2], p[3])), fmaxf(fmaxf(p[4], p[5]), fmaxf(p[6], p[7])));
    float mn = fminf(fminf(fminf(p[0], p[1]), fminf(p[2], p[3])), fminf(fminf(p[4], p[5]), fminf(p[6], p[7])));
    {
        auto r = __builtin_amdgcn_permlane32_swap(__float_as_uint(mx), __float_as_uint(mx), false, false);
        mx = fmaxf(__uint_as_float(r[0]), __uint_as_float(r[1]));
        auto s = __builtin_amdgcn_permlane32_swap(__float_as_uint(mn), __float_as_uint(mn), false, false);
        mn = fminf(__uint_as_float(s[0]), __uint_as_float(s[1]));
    }
    const float cx = -mx * LOG2E, cn = mn * LOG2E;
    float ep[8], en[8];
    float sp = 0.f, sn = 0.f;
#pragma unroll
    for (int i = 0; i < 8; ++i) {
        ep[i] = __builtin_amdgcn_exp2f(__builtin_fmaf(p[i], LOG2E, cx));
        en[i] = __builtin_amdgcn_exp2f(__builtin_fmaf(p[i], -LOG2E, cn));
        sp += ep[i]; sn += en[i];
    }
    {
        auto r = __builtin_amdgcn_permlane32_swap(__float_as_uint(sp), __float_as_uint(sp), false, false);
        sp = __uint_as_float(r[0]) + __uint_as_float(r[1]);
        auto s = __builtin_amdgcn_permlane32_swap(__float_as_uint(sn), __float_as_uint(sn), false, false);
        sn = __uint_as_float(s[0]) + __uint_as_float(s[1]);
    }
    const float rp = __builtin_amdgcn_rcpf(sp), rn = __builtin_amdgcn_rcpf(sn);
#pragma unroll
    for (int i = 0; i < 4; ++i) {
        plus[i]  = pk(ep[2 * i] * rp, ep[2 * i + 1] * rp);
        minus[i] = pk(en[2 * i] * rn, en[2 * i + 1] * rn);
    }
}

DI bf16x8 pack8(const f32x16& x, int s) {
    u32x4 v = {pk(x[8 * s], x[8 * s + 1]), pk(x[8 * s + 2], x[8 * s + 3]), pk(x[8 * s + 4], x[8 * s + 5]), pk(x[8 * s + 6], x[8 * s + 7])};
    return as_frag(v);
}

DI void p1_role(const u16* __restrict__ x, const u16* __restrict__ wqkv, const u16* __restrict__ wphi, char* __restrict__ ws, int b, lds_t smem) {
    const int lane = threadIdx.x & 63;
    const int w = __builtin_amdgcn_readfirstlane(threadIdx.x >> 6);
    const int xcd = b & 7, i = b >> 3;
    const int rt = 4 * (xcd >> 1) + (i >> 3), hp = 8 * (xcd & 1) + (i & 7);
    const int c0 = ((i & 7) + 2 * (i >> 3)) & 7;
    const int t0 = 128 * rt;
    const int stamp_base = b; (void)stamp_base;
    STAMP(0);

    unsigned xoff[4];
#pragma unroll
    for (int ii = 0; ii < 4; ++ii)
        xoff[ii] = (unsigned)((32 * w + 8 * ii + (lane >> 3)) * 1024 + (((lane & 7) ^ ((4 * ii + (lane >> 4)) & 7)) * 16));
    const unsigned woff = (unsigned)((16 * w + (lane >> 2)) * 3072 + (lane & 3) * 16);
    const char* xbase = (const char*)x + (size_t)t0 * 1024;
    const char* wbase = (const char*)wqkv + hp * 64;
    const unsigned sm = ldsaddr(smem);

    auto issue_x = [&](int c, int st) {
        const int kc = (c0 + c) & 7;
#pragma unroll
        for (int ii = 0; ii < 4; ++ii) glds16(xoff[ii], xbase + kc * 128, sm + P1_X + st * 16384 + (32 * w + 8 * ii) * 128);
    };
    auto issue_w = [&](int c, int st) {
        const int kc = (c0 + c) & 7;
#pragma unroll
        for (int j = 0; j < 3; ++j) glds16(woff, wbase + (size_t)kc * (64 * 3072) + j * 1024, sm + P1_W + st * 12288 + j * 4096 + w * 1024);
    };
    issue_w(0, 0); issue_x(0, 0); issue_x(1, 1);

    const int h = lane >> 5;
    bf16x8 wf[2];
#pragma unroll
    for (int hh = 0; hh < 2; ++hh) {
        const u16* wp = wphi + (2 * hp + hh) * 256 + (lane & 15);
#pragma unroll
        for (int j = 0; j < 8; ++j) wf[hh][j] = (short)wp[(8 * (j >> 2) + 4 * h + (j & 3)) * 16];
    }

    const int trow = 32 * w + (lane & 31);
    unsigned xrd[4];
#pragma unroll
    for (int ks = 0; ks < 4; ++ks) xrd[ks] = (unsigned)(P1_X + trow * 128 + (((2 * ks + h) ^ ((trow >> 1) & 7)) * 16));
    const unsigned wrd = (unsigned)(P1_W + (8 * h + ((lane & 15) >> 2)) * 64 + 32 * ((lane >> 4) & 1) + 8 * (lane & 3));

    f32x16 acc[3];
#pragma unroll
    for (int j = 0; j < 3; ++j)
#pragma unroll
        for (int r = 0; r < 16; ++r) acc[j][r] = 0.f;

#pragma unroll
    for (int c = 0; c < 8; ++c) {
        if (c < 7) WAIT_BAR(4); else WAIT_BAR(0);
        if (c == 0) STAMP(1);
        if (c + 1 < 8) issue_w(c + 1, (c + 1) & 1);
        if (c + 2 < 8) issue_x(c + 2, (c + 2) % 3);
#pragma unroll
        for (int ks = 0; ks < 4; ++ks) {
            const bf16x8 xb = as_frag(lds_r128(smem + xrd[ks] + (c % 3) * 16384));
#pragma unroll
            for (int j = 0; j < 3; ++j) {
                const lds_t wp = smem + wrd + (c & 1) * 12288 + j * 4096 + ks * 1024;
                const bf16x8 wa = frag2(lds_tr(wp), lds_tr(wp + 256));
                acc[j] = mfma32(wa, xb, acc[j]);
            }
        }
    }
    STAMP(2);
    LDS_BAR();

    const unsigned sg = (unsigned)((trow >> 1) & 7);
    f32x16 zero16;
#pragma unroll
    for (int r = 0; r < 16; ++r) zero16[r] = 0.f;
#pragma unroll
    for (int hh = 0; hh < 2; ++hh) {
        const f32x16 p = mfma32(wf[hh], pack8(acc[1], hh), zero16);
        u32x4 plus, minus;
        softmax_pm<false>(p, plus, minus);
        const lds_t row = smem + P1_PH + hh * 8192 + trow * 64;
#pragma unroll
        for (int ft = 0; ft < 2; ++ft) {
            u32x2 a = {plus[2 * ft], plus[2 * ft + 1]}, m = {minus[2 * ft], minus[2 * ft + 1]};
            lds_w64(row + (((4 * ft + h) ^ sg) * 8), a);
            lds_w64(row + (((4 * ft + 2 + h) ^ sg) * 8), m);
        }
        const lds_t vrow = smem + P1_V + trow * 64;
#pragma unroll
        for (int g = 0; g < 2; ++g) {
            u32x2 v = {pk(acc[2][8 * hh + 4 * g], acc[2][8 * hh + 4 * g + 1]), pk(acc[2][8 * hh + 4 * g + 2], acc[2][8 * hh + 4 * g + 3])};
            lds_w64(vrow + (((4 * hh + 2 * g + h) ^ sg) * 8), v);
        }
    }
    LDS_BAR();
    {
        const int head = w >> 1, ft = w & 1, kq = lane >> 4, q = (lane & 15) >> 2, p = lane & 3;
        f32x4 kv = {0.f, 0.f, 0.f, 0.f}, zz = {0.f, 0.f, 0.f, 0.f};
        const u32x4 onesv = {0x3F803F80u, 0x3F803F80u, 0x3F803F80u, 0x3F803F80u};
        const bf16x8 ones = as_frag(onesv);
#pragma unroll
        for (int ks = 0; ks < 4; ++ks) {
            u32x2 a[2], bb[2];
#pragma unroll
            for (int tq = 0; tq < 2; ++tq) {
                const int t = 32 * ks + 8 * kq + 4 * tq + q;
                const int s = (t >> 1) & 7;
                a[tq]  = lds_tr(smem + P1_PH + head * 8192 + t * 64 + (((4 * ft + p) ^ s) * 8));
                bb[tq] = lds_tr(smem + P1_V + t * 64 + (((4 * head + p) ^ s) * 8));
            }
            const bf16x8 af = frag2(a[0], a[1]);
            kv = mfma16(af, frag2(bb[0], bb[1]), kv);
            zz = mfma16(af, ones, zz);
        }
        const int hg = 2 * hp + head;
        *(f32x4*)(ws + WS_SLAB + ((((size_t)rt * 32 + hg) * 2 + ft) * 64 + lane) * 16) = kv;
        if ((lane & 15) == 0) *(f32x4*)(ws + WS_ZSL + ((((size_t)rt * 32 + hg) * 2 + ft) * 4 + kq) * 16) = zz;
    }
    STAMP(3);
#pragma unroll
    for (int hh = 0; hh < 2; ++hh) {
        const f32x16 p = mfma32(wf[hh], pack8(acc[0], hh), zero16);
        u32x4 plus, minus;
        softmax_pm<true>(p, plus, minus);
        const int t16 = (t0 + 32 * w + (lane & 31)) >> 4;
        char* dst = ws + WS_PHIQ + ((size_t)t16 * 32 + 2 * hp + hh) * 1024 + (h * 16 + (lane & 15)) * 16;
        *(u32x4*)dst = plus;
        *(u32x4*)(dst + 512) = minus;
    }
    STAMP(4);
}

constexpr int CV_XY = 0;
constexpr int CV_EX = 0;
constexpr int CV_KR = 8192;
constexpr int CV_Q  = 17408;
constexpr int CV_E2 = 0;
constexpr int CV_UT = 69632;
constexpr int QS = 4160;

DI f32x2 cexp_p(float a, float b, float p) {
    const float m = __builtin_amdgcn_exp2f(a * p * LOG2E);
    const float th = b * p;
    const float hi = th * 0.15915494309189535f;
    const float lo = __builtin_fmaf(th, 0.15915494309189535f, -hi) + th * 6.4206382149e-9f;
    const float fr = __builtin_amdgcn_fractf(hi) + lo;
    f32x2 r = {m * __builtin_amdgcn_cosf(fr), m * __builtin_amdgcn_sinf(fr)};
    return r;
}
struct Cm { f32x2 w, wn; };
DI Cm mk(f32x2 w) { Cm m; m.w = w; m.wn = (f32x2){-w.y, w.x}; return m; }
DI f32x2 cmul(f32x2 x, const Cm& m) { f32x2 xx = {x.x, x.x}, yy = {x.y, x.y}; return __builtin_elementwise_fma(yy, m.wn, xx * m.w); }

DI void conv_role(const u16* __restrict__ x, const u16* __restrict__ logA, const u16* __restrict__ Aim, const u16* __restrict__ Cre,
                  const u16* __restrict__ Cim, const u16* __restrict__ logdt, const u16* __restrict__ gw, char* __restrict__ ws, int b, lds_t smem) {
    const int tid = threadIdx.x, lane = tid & 63;
    const int w = __builtin_amdgcn_readfirstlane(tid >> 6);
    const int ch = w >> 1, hf = w & 1;
    const int cb = 32 * (b & 7) + (b >> 3);
    const int d = 2 * cb + ch;
    const int stamp_base = 256 + b; (void)stamp_base;
    STAMP(0);

    const int n = 64 * hf + lane;
    const unsigned la = logA[d * 128 + n], ai = Aim[d * 128 + n], cr = Cre[d * 128 + n], ci = Cim[d * 128 + n], ld = logdt[d];
    u32x4 gx[2];
#pragma unroll
    for (int r = 0; r < 2; ++r) gx[r] = *(const u32x4*)(x + (size_t)(8 * cb + 2 * w + r) * DM + lane * 8);
    const u32x4 gwv = *(const u32x4*)(gw + lane * 8);
    unsigned uv[8];
#pragma unroll
    for (int r = 0; r < 8; ++r) uv[r] = *(const unsigned*)(x + (size_t)(8 * tid + r) * DM + 2 * cb);

    {
        const float dt = rbf(expf(bf2f(ld)));
        const float a = -rbf(expf(bf2f(la))) * dt;
        const float bb = bf2f(ai) * dt;
        const f32x2 w1 = cexp_p(a, bb, 1.f), w8 = cexp_p(a, bb, 8.f), w256 = cexp_p(a, bb, 256.f);
        const lds_t xy = smem + CV_XY + w * 17408 + lane * 4;
        {
            const Cm m8 = mk(w8);
            f32x2 y = {1.f, 0.f};
            lds_w32(xy + 32 * 272, pk(y.x, y.y));
            y = w8;
            lds_w32(xy + 33 * 272, pk(y.x, y.y));
#pragma unroll
            for (int c = 2; c < 32; ++c) { y = cmul(y, m8); lds_w32(xy + (32 + c) * 272, pk(y.x, y.y)); }
        }
        {
            const f32x2 c1 = {w1.x, -w1.y}, c8 = {w8.x, -w8.y}, c256 = {w256.x, -w256.y};
            const Cm m1 = mk(c1), m8 = mk(c8), m256 = mk(c256);
            const f32x2 c2 = cmul(c1, m1);
            const Cm m2 = mk(c2);
            const Cm m4 = mk(cmul(c2, m2));
            const f32x2 x0 = {2.f * bf2f(cr) * dt, -2.f * bf2f(ci) * dt};
#define PUT(row, v) lds_w32(xy + (row) * 272, pk((v).x, (v).y))
            f32x2 v = x0; PUT(0, v);
            v = cmul(v, m1); PUT(1, v);
            v = cmul(v, m1); PUT(2, v);
            v = cmul(v, m1); PUT(3, v);
            v = cmul(v, m1); PUT(8, v);
            v = cmul(v, m1); PUT(9, v);
            v = cmul(v, m1); PUT(10, v);
            v = cmul(v, m1); PUT(11, v);
            f32x2 bse = cmul(x0, m256); PUT(4, bse);
            v = cmul(bse, m2); PUT(5, v);
            v = cmul(v, m2); PUT(6, v);
            v = cmul(v, m2); PUT(7, v);
            v = cmul(v, m2); PUT(12, v);
            bse = cmul(bse, m256); PUT(13, bse);
            v = cmul(bse, m4); PUT(14, v);
            v = cmul(v, m4); PUT(15, v);
            bse = cmul(bse, m256); PUT(20, bse);
            v = cmul(bse, m4); PUT(21, v);
            v = cmul(v, m4); PUT(22, v);
            bse = cmul(bse, m256); PUT(16, bse);
            v = cmul(bse, m8); PUT(17, v);
            bse = cmul(bse, m256); PUT(18, bse);
            v = cmul(bse, m8); PUT(19, v);
            bse = cmul(bse, m256); PUT(24, bse);
            v = cmul(bse, m8); PUT(25, v);
            bse = cmul(bse, m256); PUT(26, bse);
            v = cmul(bse, m8); PUT(27, v);
#undef PUT
        }
    }
    STAMP(1);
#pragma unroll
    for (int r = 0; r < 2; ++r) {
        float s = 0.f;
#pragma unroll
        for (int j = 0; j < 4; ++j) {
            const unsigned pr = pk(bflo(gx[r][j]) * bflo(gwv[j]), bfhi(gx[r][j]) * bfhi(gwv[j]));
            s += bflo(pr) + bfhi(pr);
        }
#pragma unroll
        for (int o = 32; o > 0; o >>= 1) s += __shfl_xor(s, o);
        const float g = rbf(1.f / (1.f + expf(-rbf(s))));
        if (lane == 0) *(float*)(ws + WS_G + (size_t)(8 * cb + 2 * w + r) * 4) = g;
    }
    {
        u32x4 u0, u1;
#pragma unroll
        for (int m = 0; m < 4; ++m) {
            u0[m] = __builtin_amdgcn_perm(uv[2 * m + 1], uv[2 * m], 0x05040100u);
            u1[m] = __builtin_amdgcn_perm(uv[2 * m + 1], uv[2 * m], 0x07060302u);
        }
        const lds_t ut = smem + CV_UT + (tid & 3) * 1280 + ((tid >> 2) + 16) * 16;
        lds_w128(ut, u0);
        lds_w128(ut + 5120, u1);
        if (hf == 0) { const u32x4 z = {0u, 0u, 0u, 0u}; lds_w128(smem + CV_UT + ch * 5120 + (lane >> 4) * 1280 + (lane & 15) * 16, z); }
    }
    const int h = lane >> 5;
    f32x16 kd;
#pragma unroll
    for (int r = 0; r < 16; ++r) kd[r] = 0.f;
    {
        asm volatile("s_waitcnt lgkmcnt(0)" ::: "memory");
        const lds_t xa = smem + CV_XY + w * 17408 + (lane & 31) * 272 + h * 16;
#pragma unroll
        for (int s = 0; s < 8; ++s)
            kd = mfma32(as_frag(lds_r128(xa + s * 32)), as_frag(lds_r128(xa + 32 * 272 + s * 32)), kd);
    }
    LDS_BAR();
    {
        const lds_t e = smem + CV_EX + w * 2048 + lane * 16;
        const int o = 8 * (1 - hf);
        f32x4 s0, s1;
        if (hf == 0) { s0 = (f32x4){kd[8], kd[9], kd[10], kd[11]}; s1 = (f32x4){kd[12], kd[13], kd[14], kd[15]}; }
        else         { s0 = (f32x4){kd[0], kd[1], kd[2], kd[3]};   s1 = (f32x4){kd[4], kd[5], kd[6], kd[7]}; }
        (void)o;
        lds_w128(e, __builtin_bit_cast(u32x4, s0));
        lds_w128(e + 1024, __builtin_bit_cast(u32x4, s1));
    }
    LDS_BAR();
    {
        const lds_t e = smem + CV_EX + (w ^ 1) * 2048 + lane * 16;
        const f32x4 s0 = __builtin_bit_cast(f32x4, lds_r128(e)), s1 = __builtin_bit_cast(f32x4, lds_r128(e + 1024));
        float k[8];
#pragma unroll
        for (int r = 0; r < 4; ++r) { k[r] = (hf == 0 ? kd[r] : kd[8 + r]) + s0[r]; k[4 + r] = (hf == 0 ? kd[4 + r] : kd[12 + r]) + s1[r]; }
        const int c = lane & 31;
        const lds_t kr = smem + CV_KR + ch * 4352 + 16;
        if (hf == 0) {
            u32x4 v;
            if (h == 0) v = (u32x4){pk(k[7], k[6]), pk(k[5], k[4]), pk(k[3], k[2]), pk(k[1], k[0])};
            else        v = (u32x4){pk(k[4], k[3]), pk(k[2], k[2]), pk(k[2], k[1]), pk(k[0], k[0])};
            lds_w128(kr + (32 * h + c) * 16, v);
            if (h == 1) { v = (u32x4){pk(k[7], k[7]), pk(k[6], k[6]), pk(k[6], k[5]), pk(k[5], k[5])}; lds_w128(kr + (64 + c) * 16, v); }
            if (lane == 0) { const u32x4 z = {0u, 0u, 0u, 0u}; lds_w128(kr - 16, z); }
        } else {
            if (h == 1) {
                const u32x4 v = {pk(k[2], k[2]), pk(k[1], k[1]), pk(k[1], k[0]), pk(k[0], k[0])};
                lds_w128(kr + (96 + c) * 16, v);
            } else {
#pragma unroll
                for (int al = 0; al < 4; ++al) {
                    const float k0 = k[2 * al], k8 = k[2 * al + 1];
                    const u32x4 v = {pk(k8, k8), pk(k8, (c & 1) ? k8 : k0), pk(k0, k0), pk(k0, k0)};
                    lds_w128(kr + (128 + 32 * al + c) * 16, v);
                }
            }
        }
    }
    LDS_BAR();
    {
        const lds_t qb = smem + CV_Q + ch * 16640;
#pragma unroll
        for (int it = 0; it < 2; ++it) {
            const int c8 = 128 * hf + 64 * it + lane;
            const lds_t kr = smem + CV_KR + ch * 4352 + c8 * 16;
            const u32x4 r = lds_r128(kr + 16);
            const u32x2 pv = *(LAS u32x2*)kr;
            const unsigned a0 = __builtin_amdgcn_alignbit(r[1], r[0], 16), a1 = __builtin_amdgcn_alignbit(r[2], r[1], 16),
                           a2 = __builtin_amdgcn_alignbit(r[3], r[2], 16), a3 = __builtin_amdgcn_alignbit(pv[0], r[3], 16),
                           a4 = __builtin_amdgcn_alignbit(pv[1], pv[0], 16);
            const lds_t q = qb + (2 * c8 + 8) * 8;
            lds_w128(q,          (u32x4){a3, a4, a1, a2});
            lds_w128(q + QS,     (u32x4){r[3], pv[0], r[1], r[2]});
            lds_w128(q + 2 * QS, (u32x4){a2, a3, a0, a1});
            lds_w128(q + 3 * QS, (u32x4){r[2], r[3], r[0], r[1]});
        }
        if (hf == 0 && lane < 16) { const u32x4 z = {0u, 0u, 0u, 0u}; lds_w128(qb + (lane >> 2) * QS + (lane & 3) * 16, z); }
    }
    LDS_BAR();
    STAMP(2);
    f32x4 acc[4][2];
#pragma unroll
    for (int nn = 0; nn < 4; ++nn)
#pragma unroll
        for (int mb = 0; mb < 2; ++mb) acc[nn][mb] = (f32x4){0.f, 0.f, 0.f, 0.f};
    {
        const int r = lane & 15, kq = lane >> 4;
        const lds_t la0 = smem + CV_Q + ch * 16640 + (r & 3) * QS + ((r >> 2) - 2 * kq + 7) * 8 + hf * 8 * 64;
        const lds_t lb0 = smem + CV_UT + ch * 5120 + kq * 1280 + (r + 16) * 16 - hf * 8 * 16;
#pragma unroll 2
        for (int dp = 0; dp < 8; ++dp) {
            bf16x8 af[4][2], bf[4];
#pragma unroll
            for (int mu = 0; mu < 4; ++mu) bf[mu] = as_frag(lds_r128(lb0 - dp * 16 + mu * 256));
#pragma unroll
            for (int m = 0; m < 4; ++m)
#pragma unroll
                for (int mb = 0; mb < 2; ++mb) {
                    const lds_t p = la0 + dp * 64 + m * 1024 + mb * 32;
                    const u32x2 f0 = lds_r64v(p + 8), f1 = lds_r64v(p);
                    af[m][mb] = frag2(f0, f1);
                }
#pragma unroll
            for (int m = 0; m < 4; ++m)
#pragma unroll
                for (int nn = m; nn < 4; ++nn)
#pragma unroll
                    for (int mb = 0; mb < 2; ++mb) acc[nn][mb] = mfma16(af[m][mb], bf[nn - m], acc[nn][mb]);
        }
    }
    STAMP(3);
    {
        f32x4 mine[2][2], other[2][2];
        if (hf == 0) {
#pragma unroll
            for (int q = 0; q < 2; ++q)
#pragma unroll
                for (int mb = 0; mb < 2; ++mb) { mine[q][mb] = acc[q][mb]; other[q][mb] = acc[2 + q][mb]; }
        } else {
#pragma unroll
            for (int q = 0; q < 2; ++q)
#pragma unroll
                for (int mb = 0; mb < 2; ++mb) { mine[q][mb] = acc[2 + q][mb]; other[q][mb] = acc[q][mb]; }
        }
        const lds_t e = smem + CV_E2 + w * 4096 + lane * 16;
#pragma unroll
        for (int q = 0; q < 2; ++q)
#pragma unroll
            for (int mb = 0; mb < 2; ++mb) lds_w128(e + (2 * q + mb) * 1024, __builtin_bit_cast(u32x4, other[q][mb]));
        LDS_BAR();
        const lds_t e2 = smem + CV_E2 + (w ^ 1) * 4096 + lane * 16;
        const int kq = lane >> 4;
#pragma unroll
        for (int q = 0; q < 2; ++q) {
            u32x2 pm[2];
#pragma unroll
            for (int mb = 0; mb < 2; ++mb) {
                const f32x4 o = __builtin_bit_cast(f32x4, lds_r128(e2 + (2 * q + mb) * 1024));
                const f32x4 v = mine[q][mb] + o;
                pm[mb] = (u32x2){pk(v[0], v[1]), pk(v[2], v[3])};
            }
            auto s0 = __builtin_amdgcn_permlane16_swap(pm[0].x, pm[1].x, false, false);
            auto s1 = __builtin_amdgcn_permlane16_swap(pm[0].y, pm[1].y, false, false);
            const u32x4 o = {s0[0], s1[0], s0[1], s1[1]};
            const int t = 512 * (2 * hf + q) + 32 * (lane & 15) + (kq & 1) * 16 + (kq >> 1) * 8;
            *(u32x4*)(ws + WS_YT + ((size_t)d * SEQ + t) * 2) = o;
        }
    }
    STAMP(4);
}

__global__ __launch_bounds__(256, 2) void k1(const u16* __restrict__ x, const u16* __restrict__ logA, const u16* __restrict__ Aim,
                                             const u16* __restrict__ Cre, const u16* __restrict__ Cim, const u16* __restrict__ logdt,
                                             const u16* __restrict__ wqkv, const u16* __restrict__ wphi, const u16* __restrict__ gw,
                                             char* __restrict__ ws) {
    __shared__ __attribute__((aligned(16))) char smem_[79872];
    const lds_t smem = (lds_t)smem_;
    const int b = blockIdx.x;
    if (b < 256) p1_role(x, wqkv, wphi, ws, b, smem);
    else conv_role(x, logA, Aim, Cre, Cim, logdt, gw, ws, b - 256, smem);
}

__global__ __launch_bounds__(64) void k2(char* __restrict__ ws) {
    const int hg = blockIdx.x, lane = threadIdx.x;
    f32x4 s[2] = {{0.f, 0.f, 0.f, 0.f}, {0.f, 0.f, 0.f, 0.f}}, z[2] = {{0.f, 0.f, 0.f, 0.f}, {0.f, 0.f, 0.f, 0.f}};
#pragma unroll
    for (int rt = 0; rt < 16; ++rt)
#pragma unroll
        for (int ft = 0; ft < 2; ++ft) {
            s[ft] += *(const f32x4*)(ws + WS_SLAB + ((((size_t)rt * 32 + hg) * 2 + ft) * 64 + lane) * 16);
            z[ft] += *(const f32x4*)(ws + WS_ZSL + ((((size_t)rt * 32 + hg) * 2 + ft) * 4 + (lane & 3)) * 16);
        }
    *(u32x4*)(ws + WS_KVF + ((size_t)hg * 64 + lane) * 16) = (u32x4){pk(s[0][0], s[0][1]), pk(s[0][2], s[0][3]), pk(s[1][0], s[1][1]), pk(s[1][2], s[1][3])};
    if (lane < 4) *(u32x4*)(ws + WS_ZF + ((size_t)hg * 4 + lane) * 16) = (u32x4){pk(z[0][0], z[0][1]), pk(z[0][2], z[0][3]), pk(z[1][0], z[1][1]), pk(z[1][2], z[1][3])};
}

constexpr int F_WO = 0, F_KV = 65536, F_Z = F_KV + 32768, F_Y = F_Z + 2048;
constexpr int F_TOTAL = F_Y + 8192;

__global__ __launch_bounds__(256, 1) void k3(const u16* __restrict__ x, const u16* __restrict__ wo, const u16* __restrict__ dskip,
                                             char* __restrict__ ws, u16* __restrict__ out) {
    __shared__ __attribute__((aligned(16))) char smem_[F_TOTAL];
    const lds_t smem = (lds_t)smem_;
    const int lane = threadIdx.x & 63;
    const int w = __builtin_amdgcn_readfirstlane(threadIdx.x >> 6);
    const int b = blockIdx.x;
    const int rt = 4 * (b & 7) + (b >> 6), ct = (b >> 3) & 7;
    const int t0 = 64 * rt, n0 = 64 * ct;
    const int stamp_base = 512 + b; (void)stamp_base;
    STAMP(0);
    const unsigned sm = ldsaddr(smem);
    {
        const char* kvb = ws + WS_KVF;
#pragma unroll
        for (int i = 0; i < 8; ++i) glds16((unsigned)(lane * 16), kvb + (w * 8 + i) * 1024, sm + F_KV + (w * 8 + i) * 1024);
        if (w < 2) glds16((unsigned)(lane * 16), ws + WS_ZF + w * 1024, sm + F_Z + w * 1024);
#pragma unroll
        for (int i = 0; i < 2; ++i) {
            const int dl = 16 * w + 8 * i + (lane >> 3);
            const unsigned vo = (unsigned)(dl * (SEQ * 2) + (((lane & 7) ^ (((dl >> 1) & 3) << 1)) * 16));
            glds16(vo, ws + WS_YT + ((size_t)n0 * SEQ + t0) * 2, sm + F_Y + (16 * w + 8 * i) * 128);
        }
#pragma unroll
        for (int i = 0; i < 16; ++i) {
            const int k = 128 * w + 8 * i + (lane >> 3);
            const unsigned vo = (unsigned)(k * 1024 + (((lane & 7) ^ (((k >> 1) & 3) << 1)) * 16));
            glds16(vo, (const char*)wo + n0 * 2, sm + F_WO + (128 * w + 8 * i) * 128);
        }
    }
    u32x4 ph[32];
    {
        const char* pq = ws + WS_PHIQ + (size_t)(4 * rt + w) * 32768 + lane * 16;
#pragma unroll
        for (int hh = 0; hh < 32; ++hh) ph[hh] = *(const u32x4*)(pq + hh * 1024);
    }
    const int tt = lane & 15, kq = lane >> 4;
    const int t = t0 + 16 * w + tt;
    u32x2 uu[4], dk[4];
#pragma unroll
    for (int nt = 0; nt < 4; ++nt) {
        uu[nt] = *(const u32x2*)(x + (size_t)t * DM + n0 + 16 * nt + 4 * kq);
        dk[nt] = *(const u32x2*)(dskip + n0 + 16 * nt + 4 * kq);
    }
    const float g = *(const float*)(ws + WS_G + (size_t)t * 4);
    WAIT_BAR(0);
    STAMP(1);

    f32x4 acc[4];
#pragma unroll
    for (int nt = 0; nt < 4; ++nt) acc[nt] = (f32x4){0.f, 0.f, 0.f, 0.f};
    const f32x4 z4 = {0.f, 0.f, 0.f, 0.f};
    const int q = tt >> 2, p = tt & 3;
#pragma unroll
    for (int hp = 0; hp < 16; ++hp) {
        u32x2 ob[2];
#pragma unroll
        for (int e = 0; e < 2; ++e) {
            const int hg = 2 * hp + e;
            const bf16x8 kvf = as_frag(lds_r128(smem + F_KV + hg * 1024 + lane * 16));
            const bf16x8 zf = as_frag(lds_r128(smem + F_Z + hg * 64 + kq * 16));
            const bf16x8 pf = as_frag(ph[hg]);
            const f32x4 num = mfma16(kvf, pf, z4);
            const f32x4 den = mfma16(zf, pf, z4);
            const float r = __builtin_amdgcn_rcpf(den[0] + 1e-6f);
            ob[e] = (u32x2){pk(num[0] * r, num[1] * r), pk(num[2] * r, num[3] * r)};
        }
        const bf16x8 of = frag2(ob[0], ob[1]);
#pragma unroll
        for (int nt = 0; nt < 4; ++nt) {
            u32x2 a[2];
#pragma unroll
            for (int tq = 0; tq < 2; ++tq) {
                const int k = 32 * hp + 16 * tq + 4 * kq + q;
                a[tq] = lds_tr(smem + F_WO + k * 128 + (((2 * nt + (p >> 1)) ^ (((k >> 1) & 3) << 1)) * 16) + 8 * (p & 1));
            }
            acc[nt] = mfma16(frag2(a[0], a[1]), of, acc[nt]);
        }
    }
    STAMP(2);
    u32x2 res[4];
#pragma unroll
    for (int nt = 0; nt < 4; ++nt) {
        const int dl = 16 * nt + 4 * kq + q;
        const int c = (16 * w + 4 * p) >> 3;
        const u32x2 yv = lds_tr(smem + F_Y + dl * 128 + ((c ^ (((dl >> 1) & 3) << 1)) * 16) + 8 * (p & 1));
        float o[4];
#pragma unroll
        for (int r = 0; r < 4; ++r) {
            const float y = (r & 1) ? bfhi(yv[r >> 1]) : bflo(yv[r >> 1]);
            const float uq = (r & 1) ? bfhi(uu[nt][r >> 1]) : bflo(uu[nt][r >> 1]);
            const float ds = (r & 1) ? bfhi(dk[nt][r >> 1]) : bflo(dk[nt][r >> 1]);
            const float a = rbf(y + rbf(uq * ds));
            const float bq = rbf(acc[nt][r]);
            o[r] = rbf(g * a) + rbf(rbf(1.f - g) * bq);
        }
        res[nt] = (u32x2){pk(o[0], o[1]), pk(o[2], o[3])};
    }
#pragma unroll
    for (int pr = 0; pr < 2; ++pr) {
        auto s0 = __builtin_amdgcn_permlane16_swap(res[2 * pr].x, res[2 * pr + 1].x, false, false);
        auto s1 = __builtin_amdgcn_permlane16_swap(res[2 * pr].y, res[2 * pr + 1].y, false, false);
        const u32x4 o = {s0[0], s1[0], s0[1], s1[1]};
        const int col = n0 + 32 * pr + (kq & 1) * 16 + (kq >> 1) * 8;
        *(u32x4*)(out + (size_t)t * DM + col) = o;
    }
    STAMP(3);
}

extern "C" void kernel_launch(void* const* d_in, const int* in_sizes, int n_in, void* d_out, int out_size, void* d_ws, size_t ws_size,
                              hipStream_t stream) {
    const u16* x = (const u16*)d_in[0];
    char* ws = (char*)d_ws;
    k1<<<512, 256, 0, stream>>>(x, (const u16*)d_in[1], (const u16*)d_in[2], (const u16*)d_in[3], (const u16*)d_in[4], (const u16*)d_in[5],
                                (const u16*)d_in[8], (const u16*)d_in[9], (const u16*)d_in[11], ws);
    k2<<<32, 64, 0, stream>>>(ws);
    k3<<<256, 256, 0, stream>>>(x, (const u16*)d_in[10], (const u16*)d_in[6], ws, (u16*)d_out);
}
```

```cpp
#include <hip/hip_runtime.h>
#include <stdint.h>

#define DI __device__ __forceinline__
#define LAS __attribute__((address_space(3)))
#define GAS __attribute__((address_space(1)))
typedef unsigned short u16;
typedef short bf16x8 __attribute__((ext_vector_type(8)));
typedef short v4i16_t __attribute__((ext_vector_type(4)));
typedef float f32x2 __attribute__((ext_vector_type(2)));
typedef float f32x4 __attribute__((ext_vector_type(4)));
typedef float f32x16 __attribute__((ext_vector_type(16)));
typedef unsigned u32x2 __attribute__((ext_vector_type(2)));
typedef unsigned u32x4 __attribute__((ext_vector_type(4)));
typedef __bf16 bf2_t __attribute__((ext_vector_type(2)));
typedef LAS char* lds_t;
typedef GAS unsigned gu32;
typedef __amdgpu_buffer_rsrc_t rsrc_t;

constexpr int SEQ = 2048, DM = 512;

constexpr unsigned WS_E    = 0;
constexpr unsigned WS_FU   = 1024;
constexpr unsigned WS_FKV  = 2048;
constexpr unsigned WS_FO   = 4096;
constexpr unsigned WS_FY   = 6144;
constexpr unsigned WS_G    = 8192;
constexpr unsigned WS_SLAB = 65536;
constexpr unsigned SLAB_B  = 4608;
constexpr unsigned WS_UX   = 2u << 20;
constexpr unsigned WS_YT   = 4u << 20;
constexpr unsigned WS_O    = 6u << 20;
constexpr unsigned WS_END  = 8u << 20;
constexpr size_t WS_STAMP = 16u << 20;

#define STAMP(k) do { } while (0)

#define SB() __builtin_amdgcn_sched_barrier(0)
#define RLX_AGENT __ATOMIC_RELAXED, __HIP_MEMORY_SCOPE_AGENT
constexpr int AUX_SC1 = 16;

DI float bf2f(unsigned v) { return __uint_as_float(v << 16); }
DI float bflo(unsigned v) { return __uint_as_float(v << 16); }
DI float bfhi(unsigned v) { return __uint_as_float(v & 0xffff0000u); }
DI unsigned pk(float lo, float hi) { f32x2 v = {lo, hi}; return __builtin_bit_cast(unsigned, __builtin_convertvector(v, bf2_t)); }
DI unsigned pk2(f32x2 v) { return __builtin_bit_cast(unsigned, __builtin_convertvector(v, bf2_t)); }
DI float rbf(float f) { return bflo(pk(f, f)); }
DI bf16x8 as_frag(u32x4 v) { return __builtin_bit_cast(bf16x8, v); }
DI bf16x8 frag2(u32x2 a, u32x2 b) { u32x4 v = {a.x, a.y, b.x, b.y}; return as_frag(v); }
DI u32x4 lds_r128(lds_t p) { return *(LAS u32x4*)p; }
DI u32x2 lds_r64v(lds_t p) { return *(volatile LAS u32x2*)p; }
DI u32x2 lds_tr(lds_t p) { return __builtin_bit_cast(u32x2, __builtin_amdgcn_ds_read_tr16_b64_v4i16((LAS v4i16_t*)p)); }
DI void lds_w128(lds_t p, u32x4 v) { *(LAS u32x4*)p = v; }
DI void lds_w64(lds_t p, u32x2 v) { *(LAS u32x2*)p = v; }
DI void lds_w32(lds_t p, unsigned v) { *(LAS unsigned*)p = v; }
DI f32x16 mfma32(bf16x8 a, bf16x8 b, f32x16 c) { return __builtin_amdgcn_mfma_f32_32x32x16_bf16(a, b, c, 0, 0, 0); }
DI f32x4 mfma16(bf16x8 a, bf16x8 b, f32x4 c) { return __builtin_amdgcn_mfma_f32_16x16x32_bf16(a, b, c, 0, 0, 0); }
DI unsigned ldsaddr(lds_t p) { return (unsigned)(size_t)p; }
DI lds_t opaque(lds_t p) { unsigned a = ldsaddr(p); asm volatile("" : "+v"(a)); return (lds_t)(size_t)a; }
DI u32x4 ld_sc1(rsrc_t rs, unsigned off) { return __builtin_amdgcn_raw_buffer_load_b128(rs, (int)off, 0, AUX_SC1); }
DI void st_sc1(rsrc_t rs, unsigned off, u32x4 v) { __builtin_amdgcn_raw_buffer_store_b128(v, rs, (int)off, 0, AUX_SC1); }

DI void glds16(unsigned voff, const void* sbase, unsigned dst) {
    unsigned keep;
    asm volatile("s_mov_b32 %0, m0\n\ts_mov_b32 m0, %3\n\ts_nop 0\n\tglobal_load_lds_dwordx4 %1, %2\n\ts_mov_b32 m0, %0"
                 : "=&s"(keep) : "v"(voff), "s"(sbase), "s"(dst) : "memory");
}
DI void glds16_sc1(unsigned voff, const void* sbase, unsigned dst) {
    unsigned keep;
    asm volatile("s_mov_b32 %0, m0\n\ts_mov_b32 m0, %3\n\ts_nop 0\n\tglobal_load_lds_dwordx4 %1, %2 sc1\n\ts_mov_b32 m0, %0"
                 : "=&s"(keep) : "v"(voff), "s"(sbase), "s"(dst) : "memory");
}
#define WAIT_BAR(n) asm volatile("s_waitcnt vmcnt(" #n ") lgkmcnt(0)\n\ts_barrier" ::: "memory")
#define LDS_BAR() asm volatile("s_waitcnt lgkmcnt(0)\n\ts_barrier" ::: "memory")

DI void wait_flags(const char* ws, unsigned off, int n, unsigned tag, int lane) {
    const gu32* f = (const gu32*)(ws + off) + (lane & (n - 1));
    for (unsigned spins = 0; spins < 200000u; ++spins) {
        const unsigned v = __hip_atomic_load(f, RLX_AGENT);
        if (__all(v == tag)) break;
        __builtin_amdgcn_s_sleep(1);
    }
    asm volatile("" ::: "memory");
}
DI void set_flag(char* ws, unsigned off, unsigned tag) { __hip_atomic_store((gu32*)(ws + off), tag, RLX_AGENT); }

constexpr float LOG2E = 1.4426950408889634f;

constexpr int P1_X = 0, P1_W = 49152, P1_WPHI = 73728;
constexpr int P1_PH = 0, P1_V = 32768;
constexpr int F_WO = 0, F_KVA = 65536, F_ZA = 67584, F_Y = 69632;

DI float max8(const f32x2* p) {
    return fmaxf(fmaxf(fmaxf(p[0].x, p[0].y), fmaxf(p[1].x, p[1].y)), fmaxf(fmaxf(p[2].x, p[2].y), fmaxf(p[3].x, p[3].y)));
}
DI float min8(const f32x2* p) {
    return fminf(fminf(fminf(p[0].x, p[0].y), fminf(p[1].x, p[1].y)), fminf(fminf(p[2].x, p[2].y), fminf(p[3].x, p[3].y)));
}
DI void softmax_pm(const f32x16& p, u32x4& plus, u32x4& minus) {
    f32x2 s[4];
#pragma unroll
    for (int i = 0; i < 4; ++i) s[i] = (f32x2){p[2 * i], p[2 * i + 1]} * LOG2E;
    float mx = max8(s), mn = min8(s);
    {
        auto r = __builtin_amdgcn_permlane32_swap(__float_as_uint(mx), __float_as_uint(mx), false, false);
        mx = fmaxf(__uint_as_float(r[0]) + 0.f, __uint_as_float(r[1]) + 0.f);
        auto q = __builtin_amdgcn_permlane32_swap(__float_as_uint(mn), __float_as_uint(mn), false, false);
        mn = fminf(__uint_as_float(q[0]) + 0.f, __uint_as_float(q[1]) + 0.f);
    }
    f32x2 ep[4], en[4];
#pragma unroll
    for (int i = 0; i < 4; ++i) {
        const f32x2 a = s[i] - mx, b = mn - s[i];
        ep[i] = (f32x2){__builtin_amdgcn_exp2f(a.x), __builtin_amdgcn_exp2f(a.y)};
        en[i] = (f32x2){__builtin_amdgcn_exp2f(b.x), __builtin_amdgcn_exp2f(b.y)};
    }
    const f32x2 sp2 = (ep[0] + ep[1]) + (ep[2] + ep[3]), sn2 = (en[0] + en[1]) + (en[2] + en[3]);
    float sp = sp2.x + sp2.y, sn = sn2.x + sn2.y;
    {
        auto r = __builtin_amdgcn_permlane32_swap(__float_as_uint(sp), __float_as_uint(sp), false, false);
        sp = __uint_as_float(r[0]) + __uint_as_float(r[1]);
        auto q = __builtin_amdgcn_permlane32_swap(__float_as_uint(sn), __float_as_uint(sn), false, false);
        sn = __uint_as_float(q[0]) + __uint_as_float(q[1]);
    }
    const float rp = __builtin_amdgcn_rcpf(sp), rn = __builtin_amdgcn_rcpf(sn);
#pragma unroll
    for (int i = 0; i < 4; ++i) { plus[i] = pk2(ep[i] * rp); minus[i] = pk2(en[i] * rn); }
}

DI bf16x8 pack8(const f32x16& x, int s) {
    u32x4 v = {pk(x[8 * s], x[8 * s + 1]), pk(x[8 * s + 2], x[8 * s + 3]), pk(x[8 * s + 4], x[8 * s + 5]), pk(x[8 * s + 6], x[8 * s + 7])};
    return as_frag(v);
}

struct GFrag { bf16x8 x, w[3]; };

DI void g_role(const u16* __restrict__ x, const u16* __restrict__ wqkv, const u16* __restrict__ wphi, const u16* __restrict__ wo,
               const u16* __restrict__ dskip, const u16* __restrict__ gw, char* __restrict__ ws, u16* __restrict__ out, int b, lds_t smem) {
    const int lane = threadIdx.x & 63;
    const int w = __builtin_amdgcn_readfirstlane(threadIdx.x >> 6);
    const int xcd = b & 7, i = b >> 3;
    const int rt = 4 * (xcd >> 1) + (i >> 3), hp = 8 * (xcd & 1) + (i & 7);
    const int c0 = ((i & 7) + 2 * (i >> 3)) & 7;
    const int t0 = 128 * rt;
    const rsrc_t rs = __builtin_amdgcn_make_buffer_rsrc((void*)ws, (short)0, (int)WS_END, 0x00020000);
    STAMP(0);
    __builtin_amdgcn_s_setprio(2);
    const unsigned tag = __hip_atomic_load((const gu32*)(ws + WS_E), RLX_AGENT) + 1u;

    unsigned xoff[4];
#pragma unroll
    for (int ii = 0; ii < 4; ++ii)
        xoff[ii] = (unsigned)((32 * w + 8 * ii + (lane >> 3)) * 1024 + (((lane & 7) ^ ((4 * ii + (lane >> 4)) & 7)) * 16));
    const unsigned woff = (unsigned)((16 * w + (lane >> 2)) * 3072 + (lane & 3) * 16);
    const char* xbase = (const char*)x + (size_t)t0 * 1024;
    const char* wbase = (const char*)wqkv + hp * 64;
    const unsigned sm = ldsaddr(smem);

    auto dma_x = [&](int c, int ii) { glds16(xoff[ii], xbase + ((c0 + c) & 7) * 128, sm + P1_X + (c % 3) * 16384 + (32 * w + 8 * ii) * 128); };
    auto dma_w = [&](int c, int j) { glds16(woff, wbase + (size_t)((c0 + c) & 7) * (64 * 3072) + j * 1024, sm + P1_W + (c & 1) * 12288 + j * 4096 + w * 1024); };
    if (w == 0) glds16((unsigned)(lane * 16), (const char*)wphi + hp * 1024, sm + P1_WPHI);
#pragma unroll
    for (int j = 0; j < 3; ++j) dma_w(0, j);
#pragma unroll
    for (int ii = 0; ii < 4; ++ii) dma_x(0, ii);
#pragma unroll
    for (int j = 0; j < 3; ++j) dma_w(1, j);
#pragma unroll
    for (int ii = 0; ii < 4; ++ii) dma_x(1, ii);
#pragma unroll
    for (int ii = 0; ii < 4; ++ii) dma_x(2, ii);

    const int h = lane >> 5;
    const int trow = 32 * w + (lane & 31);
    lds_t xrd[4];
#pragma unroll
    for (int ks = 0; ks < 4; ++ks) xrd[ks] = opaque(smem + P1_X + trow * 128 + (((2 * ks + h) ^ ((trow >> 1) & 7)) * 16));
    const lds_t wrd = opaque(smem + P1_W + (8 * h + ((lane & 15) >> 2)) * 64 + 32 * ((lane >> 4) & 1) + 8 * (lane & 3));

    f32x16 acc[3];
#pragma unroll
    for (int j = 0; j < 3; ++j)
#pragma unroll
        for (int r = 0; r < 16; ++r) acc[j][r] = 0.f;

    auto rd_x = [&](GFrag& f, int c, int ks) { f.x = as_frag(lds_r128(xrd[ks] + (c % 3) * 16384)); };
    auto rd_w = [&](GFrag& f, int c, int ks, int j) {
        const lds_t p = wrd + (c & 1) * 12288 + j * 4096 + ks * 1024;
        f.w[j] = frag2(lds_tr(p), lds_tr(p + 256));
    };
    GFrag fa, fb;
    WAIT_BAR(11);
    STAMP(1);
    rd_x(fa, 0, 0); rd_w(fa, 0, 0, 0); rd_w(fa, 0, 0, 1); rd_w(fa, 0, 0, 2);
    SB();
#define G_SLOT(cur, nxt, NC, NKS, HAVE, D0, D1, D2)                                         \
    do {                                                                                    \
        if (HAVE) { rd_x(nxt, NC, NKS); rd_w(nxt, NC, NKS, 0); }                            \
        SB();                                                                               \
        acc[0] = mfma32(cur.w[0], cur.x, acc[0]); D0;                                       \
        SB();                                                                               \
        if (HAVE) rd_w(nxt, NC, NKS, 1);                                                    \
        SB();                                                                               \
        acc[1] = mfma32(cur.w[1], cur.x, acc[1]); D1;                                       \
        SB();                                                                               \
        if (HAVE) rd_w(nxt, NC, NKS, 2);                                                    \
        SB();                                                                               \
        acc[2] = mfma32(cur.w[2], cur.x, acc[2]); D2;                                       \
        SB();                                                                               \
    } while (0)
#pragma unroll
    for (int c = 0; c < 8; ++c) {
        const bool px = c >= 1 && c + 2 < 8;
        G_SLOT(fa, fb, c, 1, true, if (px) dma_x(c + 2, 0), if (px) dma_x(c + 2, 1), (void)0);
        G_SLOT(fb, fa, c, 2, true, if (px) dma_x(c + 2, 2), if (px) dma_x(c + 2, 3), (void)0);
        G_SLOT(fa, fb, c, 3, true, (void)0, (void)0, (void)0);
        if (c < 7) {
            if (c == 0 || c == 6) { if (c == 0) WAIT_BAR(4); else WAIT_BAR(0); } else WAIT_BAR(4);
        }
        const bool pw = c + 2 < 8;
        G_SLOT(fb, fa, c + 1, 0, c < 7, if (pw) dma_w(c + 2, 0), if (pw) dma_w(c + 2, 1), if (pw) dma_w(c + 2, 2));
    }
#undef G_SLOT
    STAMP(2);

    const lds_t wph = smem + P1_WPHI + (4 * h + ((lane & 15) >> 2)) * 32 + 8 * (lane & 3);
    bf16x8 wf[2];
#pragma unroll
    for (int hh = 0; hh < 2; ++hh) wf[hh] = frag2(lds_tr(wph + hh * 512), lds_tr(wph + hh * 512 + 256));
    const unsigned sg = (unsigned)((trow >> 1) & 7);
    f32x16 zero16;
#pragma unroll
    for (int r = 0; r < 16; ++r) zero16[r] = 0.f;
    {
        const f32x16 p0 = mfma32(wf[0], pack8(acc[1], 0), zero16);
        const f32x16 p1 = mfma32(wf[1], pack8(acc[1], 1), zero16);
        const lds_t vrow = smem + P1_V + trow * 64;
#pragma unroll
        for (int hh = 0; hh < 2; ++hh)
#pragma unroll
            for (int g = 0; g < 2; ++g) {
                u32x2 v = {pk(acc[2][8 * hh + 4 * g], acc[2][8 * hh + 4 * g + 1]), pk(acc[2][8 * hh + 4 * g + 2], acc[2][8 * hh + 4 * g + 3])};
                lds_w64(vrow + (((4 * hh + 2 * g + h) ^ sg) * 8), v);
            }
#pragma unroll
        for (int hh = 0; hh < 2; ++hh) {
            u32x4 plus, minus;
            softmax_pm(hh == 0 ? p0 : p1, plus, minus);
            const lds_t row = smem + P1_PH + hh * 8192 + trow * 64;
#pragma unroll
            for (int ft = 0; ft < 2; ++ft) {
                lds_w64(row + (((4 * ft + h) ^ sg) * 8), (u32x2){plus[2 * ft], plus[2 * ft + 1]});
                lds_w64(row + (((4 * ft + 2 + h) ^ sg) * 8), (u32x2){minus[2 * ft], minus[2 * ft + 1]});
            }
        }
    }
    LDS_BAR();
    const int head = w >> 1, ft = w & 1, kq = lane >> 4, q = (lane & 15) >> 2, p = lane & 3;
    {
        f32x4 kv = {0.f, 0.f, 0.f, 0.f}, zz = {0.f, 0.f, 0.f, 0.f};
        const u32x4 onesv = {0x3F803F80u, 0x3F803F80u, 0x3F803F80u, 0x3F803F80u};
        const bf16x8 ones = as_frag(onesv);
        u32x2 a[4][2], bb[4][2];
#pragma unroll
        for (int ks = 0; ks < 4; ++ks)
#pragma unroll
            for (int tq = 0; tq < 2; ++tq) {
                const int t = 32 * ks + 8 * kq + 4 * tq + q;
                const int s = (t >> 1) & 7;
                a[ks][tq]  = lds_tr(smem + P1_PH + head * 8192 + t * 64 + (((4 * ft + p) ^ s) * 8));
                bb[ks][tq] = lds_tr(smem + P1_V + t * 64 + (((4 * head + p) ^ s) * 8));
            }
#pragma unroll
        for (int ks = 0; ks < 4; ++ks) {
            const bf16x8 af = frag2(a[ks][0], a[ks][1]);
            kv = mfma16(af, frag2(bb[ks][0], bb[ks][1]), kv);
            zz = mfma16(af, ones, zz);
        }
        const unsigned sb = WS_SLAB + (unsigned)(hp * 16 + rt) * SLAB_B;
        st_sc1(rs, sb + w * 1024 + lane * 16, __builtin_bit_cast(u32x4, kv));
        if ((lane & 15) == 0) st_sc1(rs, sb + 4096 + w * 64 + kq * 16, __builtin_bit_cast(u32x4, zz));
    }
    WAIT_BAR(0);
    if (threadIdx.x == 0) set_flag(ws, WS_FKV + hp * 128 + rt * 4, tag);
    STAMP(3);

    const int rt64 = 2 * rt + (hp >> 3), ct = hp & 7;
    const int ft0 = 64 * rt64, n0 = 64 * ct;
#pragma unroll
    for (int ii = 0; ii < 16; ++ii) {
        const int k = 128 * w + 8 * ii + (lane >> 3);
        glds16((unsigned)(k * 1024 + (((lane & 7) ^ (((k >> 1) & 3) << 1)) * 16)), (const char*)wo + n0 * 2, sm + F_WO + (128 * w + 8 * ii) * 128);
    }
    u32x4 gx[2];
#pragma unroll
    for (int r = 0; r < 2; ++r) gx[r] = *(const u32x4*)(x + (size_t)(t0 + 8 * hp + 2 * w + r) * DM + lane * 8);
    const u32x4 gwv = *(const u32x4*)(gw + lane * 8);
    u32x4 qp[2], qm[2];
    {
        const f32x16 p0 = mfma32(wf[0], pack8(acc[0], 0), zero16);
        const f32x16 p1 = mfma32(wf[1], pack8(acc[0], 1), zero16);
        softmax_pm(p0, qp[0], qm[0]);
        softmax_pm(p1, qp[1], qm[1]);
    }
    float gate[2];
#pragma unroll
    for (int r = 0; r < 2; ++r) {
        float s = 0.f;
#pragma unroll
        for (int j = 0; j < 4; ++j) {
            const unsigned pr = pk(bflo(gx[r][j]) * bflo(gwv[j]), bfhi(gx[r][j]) * bfhi(gwv[j]));
            s += bflo(pr) + bfhi(pr);
        }
#pragma unroll
        for (int o = 32; o > 0; o >>= 1) s += __shfl_xor(s, o);
        gate[r] = rbf(1.f / (1.f + expf(-rbf(s))));
    }
    STAMP(4);
    if (w == 0) wait_flags(ws, WS_FKV + hp * 128, 16, tag, lane);
    asm volatile("s_barrier" ::: "memory");
    STAMP(5);
    {
        u32x4 sl[16];
        const unsigned sb = WS_SLAB + (unsigned)(hp * 16) * SLAB_B;
#pragma unroll
        for (int r = 0; r < 16; ++r) sl[r] = ld_sc1(rs, sb + r * SLAB_B + w * 1024 + lane * 16);
        const u32x4 zl = ld_sc1(rs, sb + (lane >> 2) * SLAB_B + 4096 + w * 64 + (lane & 3) * 16);
        f32x4 kv = __builtin_bit_cast(f32x4, sl[0]);
#pragma unroll
        for (int r = 1; r < 16; ++r) kv += __builtin_bit_cast(f32x4, sl[r]);
        f32x4 z = __builtin_bit_cast(f32x4, zl);
#pragma unroll
        for (int o = 4; o < 64; o <<= 1) {
#pragma unroll
            for (int e = 0; e < 4; ++e) z[e] += __shfl_xor(z[e], o);
        }
        lds_w64(smem + F_KVA + ((((head * 2 + (kq >> 1)) * 2 + (kq & 1)) * 16 + (lane & 15)) * 2 + ft) * 8, (u32x2){pk(kv[0], kv[1]), pk(kv[2], kv[3])});
        if (lane < 4) lds_w64(smem + F_ZA + (((head * 2 + (lane >> 1)) * 2 + (lane & 1)) * 2 + ft) * 8, (u32x2){pk(z[0], z[1]), pk(z[2], z[3])});
    }
    LDS_BAR();
    STAMP(6);
    {
        u32x2 oa[2][2];
#pragma unroll
        for (int hh = 0; hh < 2; ++hh) {
            bf16x8 af[2];
#pragma unroll
            for (int sn = 0; sn < 2; ++sn) {
                const int r = lane & 31;
                const lds_t pa = r < 16 ? smem + F_KVA + (((hh * 2 + sn) * 2 + h) * 16 + r) * 16 : smem + F_ZA + ((hh * 2 + sn) * 2 + h) * 16;
                af[sn] = as_frag(lds_r128(pa));
            }
            f32x16 d = mfma32(af[0], as_frag(qp[hh]), zero16);
            d = mfma32(af[1], as_frag(qm[hh]), d);
            const float r = __builtin_amdgcn_rcpf(d[8] + 1e-6f);
            oa[hh][0] = (u32x2){pk(d[0] * r, d[1] * r), pk(d[2] * r, d[3] * r)};
            oa[hh][1] = (u32x2){pk(d[4] * r, d[5] * r), pk(d[6] * r, d[7] * r)};
        }
        const unsigned t16 = (unsigned)(t0 + trow) >> 4;
        const unsigned ob = WS_O + (t16 * 16 + hp) * 1024 + (h * 16 + (lane & 15)) * 16;
        st_sc1(rs, ob, (u32x4){oa[0][0].x, oa[0][0].y, oa[1][0].x, oa[1][0].y});
        st_sc1(rs, ob + 512, (u32x4){oa[0][1].x, oa[0][1].y, oa[1][1].x, oa[1][1].y});
        if (lane == 0) {
            GAS f32x2* gp = (GAS f32x2*)(ws + WS_G + (rt * 16 + hp) * 128 + w * 8);
            __hip_atomic_store((GAS unsigned long long*)gp, ((unsigned long long)__float_as_uint(gate[1]) << 32) | __float_as_uint(gate[0]), RLX_AGENT);
        }
    }
    WAIT_BAR(0);
    if (threadIdx.x == 0) set_flag(ws, WS_FO + rt * 128 + hp * 4, tag);
    STAMP(7);
    const int tt = lane & 15;
    const int t = ft0 + 16 * w + tt;
    u32x2 uu[4], dk[4];
#pragma unroll
    for (int nt = 0; nt < 4; ++nt) {
        uu[nt] = *(const u32x2*)(x + (size_t)t * DM + n0 + 16 * nt + 4 * kq);
        dk[nt] = *(const u32x2*)(dskip + n0 + 16 * nt + 4 * kq);
    }
    if (w == 1) wait_flags(ws, WS_FY + ct * 128, 32, tag, lane);
    if (w == 0) wait_flags(ws, WS_FO + (rt64 >> 1) * 128, 16, tag, lane);
    asm volatile("s_barrier" ::: "memory");
    STAMP(8);
    u32x4 of[16];
    {
        const unsigned ob = WS_O + (unsigned)(4 * rt64 + w) * 16384 + lane * 16;
#pragma unroll
        for (int k = 0; k < 16; ++k) of[k] = ld_sc1(rs, ob + k * 1024);
    }
#pragma unroll
    for (int ii = 0; ii < 2; ++ii) {
        const int dl = 16 * w + 8 * ii + (lane >> 3);
        glds16_sc1((unsigned)(dl * (SEQ * 2) + (((lane & 7) ^ (((dl >> 1) & 3) << 1)) * 16)), ws + WS_YT + ((size_t)n0 * SEQ + ft0) * 2,
                   sm + F_Y + (16 * w + 8 * ii) * 128);
    }
    const int tl = t & 127;
    const float g = __uint_as_float(__hip_atomic_load((const gu32*)(ws + WS_G + ((rt64 >> 1) * 16 + (tl >> 3)) * 128 + (tl & 7) * 4), RLX_AGENT));
    f32x4 fa4[4];
#pragma unroll
    for (int nt = 0; nt < 4; ++nt) fa4[nt] = (f32x4){0.f, 0.f, 0.f, 0.f};
    {
        lds_t wp[4];
#pragma unroll
        for (int nt = 0; nt < 4; ++nt) {
            const int k = 4 * kq + q;
            wp[nt] = opaque(smem + F_WO + k * 128 + (((2 * nt + (p >> 1)) ^ (((k >> 1) & 3) << 1)) * 16) + 8 * (p & 1));
        }
        bf16x8 wa[2][4];
#pragma unroll
        for (int nt = 0; nt < 4; ++nt) wa[0][nt] = frag2(lds_tr(wp[nt]), lds_tr(wp[nt] + 2048));
#pragma unroll
        for (int k = 0; k < 16; ++k) {
            SB();
            if (k + 1 < 16) {
#pragma unroll
                for (int nt = 0; nt < 4; ++nt) wa[(k + 1) & 1][nt] = frag2(lds_tr(wp[nt] + (k + 1) * 4096), lds_tr(wp[nt] + (k + 1) * 4096 + 2048));
            }
            SB();
#pragma unroll
            for (int nt = 0; nt < 4; ++nt) fa4[nt] = mfma16(wa[k & 1][nt], as_frag(of[k]), fa4[nt]);
        }
    }
    STAMP(9);
    WAIT_BAR(0);
    u32x2 res[4];
#pragma unroll
    for (int nt = 0; nt < 4; ++nt) {
        const int dl = 16 * nt + 4 * kq + q;
        const int c = (16 * w + 4 * p) >> 3;
        const u32x2 yv = lds_tr(smem + F_Y + dl * 128 + ((c ^ (((dl >> 1) & 3) << 1)) * 16) + 8 * (p & 1));
        float o[4];
#pragma unroll
        for (int r = 0; r < 4; ++r) {
            const float y = (r & 1) ? bfhi(yv[r >> 1]) : bflo(yv[r >> 1]);
            const float uq = (r & 1) ? bfhi(uu[nt][r >> 1]) : bflo(uu[nt][r >> 1]);
            const float ds = (r & 1) ? bfhi(dk[nt][r >> 1]) : bflo(dk[nt][r >> 1]);
            const float a = rbf(y + rbf(uq * ds));
            const float bq = rbf(fa4[nt][r]);
            o[r] = rbf(g * a) + rbf(rbf(1.f - g) * bq);
        }
        res[nt] = (u32x2){pk(o[0], o[1]), pk(o[2], o[3])};
    }
#pragma unroll
    for (int pr = 0; pr < 2; ++pr) {
        auto s0 = __builtin_amdgcn_permlane16_swap(res[2 * pr].x, res[2 * pr + 1].x, false, false);
        auto s1 = __builtin_amdgcn_permlane16_swap(res[2 * pr].y, res[2 * pr + 1].y, false, false);
        const u32x4 o = {s0[0], s1[0], s0[1], s1[1]};
        const int col = n0 + 32 * pr + (kq & 1) * 16 + (kq >> 1) * 8;
        *(u32x4*)(out + (size_t)t * DM + col) = o;
    }
    STAMP(10);
}

constexpr int CV_XY = 0;
constexpr int CV_EX = 0;
constexpr int CV_KR = 8192;
constexpr int CV_Q  = 17408;
constexpr int CV_E2 = 0;
constexpr int CV_UT = 69632;
constexpr int QS = 4160;

DI f32x2 cexp_p(float a, float b, float p) {
    const float m = __builtin_amdgcn_exp2f(a * p * LOG2E);
    const float th = b * p;
    const float hi = th * 0.15915494309189535f;
    const float lo = __builtin_fmaf(th, 0.15915494309189535f, -hi) + th * 6.4206382149e-9f;
    const float fr = __builtin_amdgcn_fractf(hi) + lo;
    f32x2 r = {m * __builtin_amdgcn_cosf(fr), m * __builtin_amdgcn_sinf(fr)};
    return r;
}
struct Cm { f32x2 w, wn; };
DI Cm mk(f32x2 w) { Cm m; m.w = w; m.wn = (f32x2){-w.y, w.x}; return m; }
DI f32x2 cmul(f32x2 x, const Cm& m) { f32x2 xx = {x.x, x.x}, yy = {x.y, x.y}; return __builtin_elementwise_fma(yy, m.wn, xx * m.w); }

struct CFrag { bf16x8 a[4][2], b[4]; };

DI void c_role(const u16* __restrict__ x, const u16* __restrict__ logA, const u16* __restrict__ Aim, const u16* __restrict__ Cre,
               const u16* __restrict__ Cim, const u16* __restrict__ logdt, char* __restrict__ ws, int b, lds_t smem) {
    const int tid = threadIdx.x, lane = tid & 63;
    const int w = __builtin_amdgcn_readfirstlane(tid >> 6);
    const int ch = w >> 1, hf = w & 1;
    const int cg = b & 7, ci_ = b >> 3;
    const int cb = 32 * cg + ci_;
    const int d = 2 * cb + ch;
    const rsrc_t rs = __builtin_amdgcn_make_buffer_rsrc((void*)ws, (short)0, (int)WS_END, 0x00020000);
    STAMP(0);
    const unsigned tag = __hip_atomic_load((const gu32*)(ws + WS_E), RLX_AGENT) + 1u;

    u32x4 xt[2];
#pragma unroll
    for (int s = 0; s < 2; ++s) xt[s] = *(const u32x4*)(x + (size_t)(64 * ci_ + 32 * s + (tid >> 3)) * DM + 64 * cg + 8 * (tid & 7));
    const int n = 64 * hf + lane;
    const unsigned la = logA[d * 128 + n], ai = Aim[d * 128 + n], cr = Cre[d * 128 + n], ci = Cim[d * 128 + n], ld = logdt[d];
#pragma unroll
    for (int s = 0; s < 2; ++s) lds_w128(smem + CV_UT + (32 * s + (tid >> 3)) * 128 + (tid & 7) * 16, xt[s]);
    LDS_BAR();
    {
        const int g = lane >> 4, q = (lane & 15) >> 2, p = lane & 3;
#pragma unroll
        for (int s = 0; s < 2; ++s) {
            const lds_t a = smem + CV_UT + (8 * (g + 4 * s) + q) * 128 + 32 * w + 8 * p;
            const u32x2 lo = lds_tr(a), hi = lds_tr(a + 512);
            st_sc1(rs, WS_UX + (cg * 32 + ci_) * 8192 + (16 * w + (lane & 15)) * 128 + (g + 4 * s) * 16, (u32x4){lo.x, lo.y, hi.x, hi.y});
        }
    }
    WAIT_BAR(0);
    if (tid == 0) set_flag(ws, WS_FU + cg * 128 + ci_ * 4, tag);
    STAMP(1);

    {
        const float dt = rbf(expf(bf2f(ld)));
        const float a = -rbf(expf(bf2f(la))) * dt;
        const float bb = bf2f(ai) * dt;
        const f32x2 w1 = cexp_p(a, bb, 1.f), w8 = cexp_p(a, bb, 8.f), w256 = cexp_p(a, bb, 256.f);
        const lds_t xy = smem + CV_XY + w * 17408 + lane * 4;
        {
            const Cm m8 = mk(w8);
            f32x2 y = {1.f, 0.f};
            lds_w32(xy + 32 * 272, pk2(y));
            y = w8;
            lds_w32(xy + 33 * 272, pk2(y));
#pragma unroll
            for (int c = 2; c < 32; ++c) { y = cmul(y, m8); lds_w32(xy + (32 + c) * 272, pk2(y)); }
        }
        {
            const f32x2 c1 = {w1.x, -w1.y}, c8 = {w8.x, -w8.y}, c256 = {w256.x, -w256.y};
            const Cm m1 = mk(c1), m8 = mk(c8), m256 = mk(c256);
            const f32x2 c2 = cmul(c1, m1);
            const Cm m2 = mk(c2);
            const Cm m4 = mk(cmul(c2, m2));
            const f32x2 x0 = {2.f * bf2f(cr) * dt, -2.f * bf2f(ci) * dt};
#define PUT(row, v) lds_w32(xy + (row) * 272, pk2(v))
            f32x2 v = x0; PUT(0, v);
            v = cmul(v, m1); PUT(1, v);
            v = cmul(v, m1); PUT(2, v);
            v = cmul(v, m1); PUT(3, v);
            v = cmul(v, m1); PUT(8, v);
            v = cmul(v, m1); PUT(9, v);
            v = cmul(v, m1); PUT(10, v);
            v = cmul(v, m1); PUT(11, v);
            f32x2 bse = cmul(x0, m256); PUT(4, bse);
            v = cmul(bse, m2); PUT(5, v);
            v = cmul(v, m2); PUT(6, v);
            v = cmul(v, m2); PUT(7, v);
            v = cmul(v, m2); PUT(12, v);
            bse = cmul(bse, m256); PUT(13, bse);
            v = cmul(bse, m4); PUT(14, v);
            v = cmul(v, m4); PUT(15, v);
            bse = cmul(bse, m256); PUT(20, bse);
            v = cmul(bse, m4); PUT(21, v);
            v = cmul(v, m4); PUT(22, v);
            bse = cmul(bse, m256); PUT(16, bse);
            v = cmul(bse, m8); PUT(17, v);
            bse = cmul(bse, m256); PUT(18, bse);
            v = cmul(bse, m8); PUT(19, v);
            bse = cmul(bse, m256); PUT(24, bse);
            v = cmul(bse, m8); PUT(25, v);
            bse = cmul(bse, m256); PUT(26, bse);
            v = cmul(bse, m8); PUT(27, v);
#undef PUT
        }
    }
    STAMP(2);
    const int h = lane >> 5;
    f32x16 kd;
#pragma unroll
    for (int r = 0; r < 16; ++r) kd[r] = 0.f;
    {
        asm volatile("s_waitcnt lgkmcnt(0)" ::: "memory");
        const lds_t xa = smem + CV_XY + w * 17408 + (lane & 31) * 272 + h * 16;
        u32x4 fa[8], fb[8];
#pragma unroll
        for (int s = 0; s < 8; ++s) { fa[s] = lds_r128(xa + s * 32); fb[s] = lds_r128(xa + 32 * 272 + s * 32); }
#pragma unroll
        for (int s = 0; s < 8; ++s) kd = mfma32(as_frag(fa[s]), as_frag(fb[s]), kd);
    }
    if (w == 0) wait_flags(ws, WS_FU + cg * 128, 32, tag, lane);
    LDS_BAR();
    STAMP(3);
    u32x4 ux[2];
#pragma unroll
    for (int s = 0; s < 2; ++s) ux[s] = ld_sc1(rs, WS_UX + (cg * 32 + 16 * s + (tid >> 4)) * 8192 + ci_ * 256 + (tid & 15) * 16);
    {
        const lds_t e = smem + CV_EX + w * 2048 + lane * 16;
        f32x4 s0, s1;
        if (hf == 0) { s0 = (f32x4){kd[8], kd[9], kd[10], kd[11]}; s1 = (f32x4){kd[12], kd[13], kd[14], kd[15]}; }
        else         { s0 = (f32x4){kd[0], kd[1], kd[2], kd[3]};   s1 = (f32x4){kd[4], kd[5], kd[6], kd[7]}; }
        lds_w128(e, __builtin_bit_cast(u32x4, s0));
        lds_w128(e + 1024, __builtin_bit_cast(u32x4, s1));
    }
    LDS_BAR();
    {
        const lds_t e = smem + CV_EX + (w ^ 1) * 2048 + lane * 16;
        const f32x4 s0 = __builtin_bit_cast(f32x4, lds_r128(e)), s1 = __builtin_bit_cast(f32x4, lds_r128(e + 1024));
        float k[8];
#pragma unroll
        for (int r = 0; r < 4; ++r) { k[r] = (hf == 0 ? kd[r] : kd[8 + r]) + s0[r]; k[4 + r] = (hf == 0 ? kd[4 + r] : kd[12 + r]) + s1[r]; }
        const int c = lane & 31;
        const lds_t kr = smem + CV_KR + ch * 4352 + 16;
        if (hf == 0) {
            u32x4 v;
            if (h == 0) v = (u32x4){pk(k[7], k[6]), pk(k[5], k[4]), pk(k[3], k[2]), pk(k[1], k[0])};
            else        v = (u32x4){pk(k[4], k[3]), pk(k[2], k[2]), pk(k[2], k[1]), pk(k[0], k[0])};
            lds_w128(kr + (32 * h + c) * 16, v);
            if (h == 1) { v = (u32x4){pk(k[7], k[7]), pk(k[6], k[6]), pk(k[6], k[5]), pk(k[5], k[5])}; lds_w128(kr + (64 + c) * 16, v); }
            if (lane == 0) { const u32x4 z = {0u, 0u, 0u, 0u}; lds_w128(kr - 16, z); }
        } else {
            if (h == 1) {
                const u32x4 v = {pk(k[2], k[2]), pk(k[1], k[1]), pk(k[1], k[0]), pk(k[0], k[0])};
                lds_w128(kr + (96 + c) * 16, v);
            } else {
#pragma unroll
                for (int al = 0; al < 4; ++al) {
                    const float k0 = k[2 * al], k8 = k[2 * al + 1];
                    const u32x4 v = {pk(k8, k8), pk(k8, (c & 1) ? k8 : k0), pk(k0, k0), pk(k0, k0)};
                    lds_w128(kr + (128 + 32 * al + c) * 16, v);
                }
            }
        }
    }
    LDS_BAR();
    {
        const lds_t qb = smem + CV_Q + ch * 16640;
#pragma unroll
        for (int it = 0; it < 2; ++it) {
            const int c8 = 128 * hf + 64 * it + lane;
            const lds_t kr = smem + CV_KR + ch * 4352 + c8 * 16;
            const u32x4 r = lds_r128(kr + 16);
            const u32x2 pv = *(LAS u32x2*)kr;
            const unsigned a0 = __builtin_amdgcn_alignbit(r[1], r[0], 16), a1 = __builtin_amdgcn_alignbit(r[2], r[1], 16),
                           a2 = __builtin_amdgcn_alignbit(r[3], r[2], 16), a3 = __builtin_amdgcn_alignbit(pv[0], r[3], 16),
                           a4 = __builtin_amdgcn_alignbit(pv[1], pv[0], 16);
            const lds_t q = qb + (2 * c8 + 8) * 8;
            lds_w128(q,          (u32x4){a3, a4, a1, a2});
            lds_w128(q + QS,     (u32x4){r[3], pv[0], r[1], r[2]});
            lds_w128(q + 2 * QS, (u32x4){a2, a3, a0, a1});
            lds_w128(q + 3 * QS, (u32x4){r[2], r[3], r[0], r[1]});
        }
        const u32x4 z = {0u, 0u, 0u, 0u};
        if (hf == 0 && lane < 16) lds_w128(qb + (lane >> 2) * QS + (lane & 3) * 16, z);
        if (hf == 1) lds_w128(smem + CV_UT + ch * 5120 + (lane >> 4) * 1280 + (lane & 15) * 16, z);
#pragma unroll
        for (int s = 0; s < 2; ++s) {
            const int rtile = 16 * s + (tid >> 4), piece = tid & 15, chunk = piece & 7;
            lds_w128(smem + CV_UT + (piece >> 3) * 5120 + (chunk & 3) * 1280 + (2 * rtile + (chunk >> 2) + 16) * 16, ux[s]);
        }
    }
    LDS_BAR();
    STAMP(4);
    f32x4 acc[4][2];
#pragma unroll
    for (int nn = 0; nn < 4; ++nn)
#pragma unroll
        for (int mb = 0; mb < 2; ++mb) acc[nn][mb] = (f32x4){0.f, 0.f, 0.f, 0.f};
    {
        const int r = lane & 15, kq = lane >> 4;
        const lds_t la0 = opaque(smem + CV_Q + ch * 16640 + (r & 3) * QS + ((r >> 2) - 2 * kq + 7) * 8 + hf * 8 * 64);
        const lds_t lb0 = opaque(smem + CV_UT + ch * 5120 + kq * 1280 + (r + 16) * 16 - hf * 8 * 16 - 7 * 16);
        auto rd_a = [&](CFrag& f, int dp, int m, int mb) {
            const lds_t p = la0 + dp * 64 + m * 1024 + mb * 32;
            const u32x2 f0 = lds_r64v(p + 8), f1 = lds_r64v(p);
            f.a[m][mb] = frag2(f0, f1);
        };
        auto rd_b = [&](CFrag& f, int dp, int mu) { f.b[mu] = as_frag(lds_r128(lb0 + (7 - dp) * 16 + mu * 256)); };
        CFrag fa, fb;
#pragma unroll
        for (int mu = 0; mu < 4; ++mu) rd_b(fa, 0, mu);
#pragma unroll
        for (int m = 0; m < 4; ++m) { rd_a(fa, 0, m, 0); rd_a(fa, 0, m, 1); }
        SB();
#define C_MM(cur, m, nn, mb) acc[nn][mb] = mfma16(cur.a[m][mb], cur.b[(nn) - (m)], acc[nn][mb])
#define C_STEP(cur, nxt, dp, HAVE)                                                                     \
        do {                                                                                           \
            if (HAVE) { rd_b(nxt, dp, 0); rd_b(nxt, dp, 1); }                                          \
            SB(); C_MM(cur, 0, 0, 0); C_MM(cur, 0, 0, 1); SB();                                        \
            if (HAVE) { rd_b(nxt, dp, 2); rd_b(nxt, dp, 3); }                                          \
            SB(); C_MM(cur, 0, 1, 0); C_MM(cur, 0, 1, 1); SB();                                        \
            if (HAVE) rd_a(nxt, dp, 0, 0);                                                             \
            SB(); C_MM(cur, 0, 2, 0); C_MM(cur, 0, 2, 1); SB();                                        \
            if (HAVE) rd_a(nxt, dp, 0, 1);                                                             \
            SB(); C_MM(cur, 0, 3, 0); C_MM(cur, 0, 3, 1); SB();                                        \
            if (HAVE) rd_a(nxt, dp, 1, 0);                                                             \
            SB(); C_MM(cur, 1, 1, 0); C_MM(cur, 1, 1, 1); SB();                                        \
            if (HAVE) rd_a(nxt, dp, 1, 1);                                                             \
            SB(); C_MM(cur, 1, 2, 0); C_MM(cur, 1, 2, 1); SB();                                        \
            if (HAVE) rd_a(nxt, dp, 2, 0);                                                             \
            SB(); C_MM(cur, 1, 3, 0); C_MM(cur, 1, 3, 1); SB();                                        \
            if (HAVE) rd_a(nxt, dp, 2, 1);                                                             \
            SB(); C_MM(cur, 2, 2, 0); C_MM(cur, 2, 2, 1); SB();                                        \
            if (HAVE) rd_a(nxt, dp, 3, 0);                                                             \
            SB(); C_MM(cur, 2, 3, 0); C_MM(cur, 2, 3, 1); SB();                                        \
            if (HAVE) rd_a(nxt, dp, 3, 1);                                                             \
            SB(); C_MM(cur, 3, 3, 0); C_MM(cur, 3, 3, 1); SB();                                        \
        } while (0)
#pragma unroll
        for (int dp = 0; dp < 8; dp += 2) {
            C_STEP(fa, fb, dp + 1, true);
            C_STEP(fb, fa, dp + 2, dp + 2 < 8);
        }
#undef C_STEP
#undef C_MM
    }
    STAMP(5);
    {
        f32x4 mine[2][2], other[2][2];
        if (hf == 0) {
#pragma unroll
            for (int q = 0; q < 2; ++q)
#pragma unroll
                for (int mb = 0; mb < 2; ++mb) { mine[q][mb] = acc[q][mb]; other[q][mb] = acc[2 + q][mb]; }
        } else {
#pragma unroll
            for (int q = 0; q < 2; ++q)
#pragma unroll
                for (int mb = 0; mb < 2; ++mb) { mine[q][mb] = acc[2 + q][mb]; other[q][mb] = acc[q][mb]; }
        }
        const lds_t e = smem + CV_E2 + w * 4096 + lane * 16;
#pragma unroll
        for (int q = 0; q < 2; ++q)
#pragma unroll
            for (int mb = 0; mb < 2; ++mb) lds_w128(e + (2 * q + mb) * 1024, __builtin_bit_cast(u32x4, other[q][mb]));
        LDS_BAR();
        const lds_t e2 = smem + CV_E2 + (w ^ 1) * 4096 + lane * 16;
        const int kq = lane >> 4;
#pragma unroll
        for (int q = 0; q < 2; ++q) {
            u32x2 pm[2];
#pragma unroll
            for (int mb = 0; mb < 2; ++mb) {
                const f32x4 o = __builtin_bit_cast(f32x4, lds_r128(e2 + (2 * q + mb) * 1024));
                const f32x4 v = mine[q][mb] + o;
                pm[mb] = (u32x2){pk(v[0], v[1]), pk(v[2], v[3])};
            }
            auto s0 = __builtin_amdgcn_permlane16_swap(pm[0].x, pm[1].x, false, false);
            auto s1 = __builtin_amdgcn_permlane16_swap(pm[0].y, pm[1].y, false, false);
            const int t = 512 * (2 * hf + q) + 32 * (lane & 15) + (kq & 1) * 16 + (kq >> 1) * 8;
            st_sc1(rs, WS_YT + (unsigned)(d * SEQ + t) * 2, (u32x4){s0[0], s1[0], s0[1], s1[1]});
        }
    }
    WAIT_BAR(0);
    if (tid == 0) set_flag(ws, WS_FY + cg * 128 + ci_ * 4, tag);
    STAMP(6);
    if (b == 0 && w == 0) {
#pragma unroll 1
        for (int g = 0; g < 8; ++g) wait_flags(ws, WS_FU + g * 128, 32, tag, lane);
#pragma unroll 1
        for (int g = 0; g < 16; ++g) wait_flags(ws, WS_FKV + g * 128, 16, tag, lane);
        if (lane == 0) set_flag(ws, WS_E, tag);
    }
}

__global__ __launch_bounds__(256, 2) void fused_mix(const u16* __restrict__ x, const u16* __restrict__ logA, const u16* __restrict__ Aim,
                                                    const u16* __restrict__ Cre, const u16* __restrict__ Cim, const u16* __restrict__ logdt,
                                                    const u16* __restrict__ dskip, const u16* __restrict__ wqkv, const u16* __restrict__ wphi,
                                                    const u16* __restrict__ wo, const u16* __restrict__ gw, char* __restrict__ ws,
                                                    u16* __restrict__ out) {
    __shared__ __attribute__((aligned(16))) char smem_[79872];
    const lds_t smem = (lds_t)smem_;
    const int b = blockIdx.x;
    if (b < 256) g_role(x, wqkv, wphi, wo, dskip, gw, ws, out, b, smem);
    else c_role(x, logA, Aim, Cre, Cim, logdt, ws, b - 256, smem);
}

extern "C" void kernel_launch(void* const* d_in, const int* in_sizes, int n_in, void* d_out, int out_size, void* d_ws, size_t ws_size,
                              hipStream_t stream) {
    fused_mix<<<512, 256, 0, stream>>>((const u16*)d_in[0], (const u16*)d_in[1], (const u16*)d_in[2], (const u16*)d_in[3], (const u16*)d_in[4],
                                       (const u16*)d_in[5], (const u16*)d_in[6], (const u16*)d_in[8], (const u16*)d_in[9], (const u16*)d_in[10],
                                       (const u16*)d_in[11], (char*)d_ws, (u16*)d_out);
}
```

```cpp
#include <hip/hip_runtime.h>
#include <stdint.h>

#define DI __device__ __forceinline__
#define LAS __attribute__((address_space(3)))
#define GAS __attribute__((address_space(1)))
typedef unsigned short u16;
typedef short bf16x8 __attribute__((ext_vector_type(8)));
typedef short v4i16_t __attribute__((ext_vector_type(4)));
typedef float f32x2 __attribute__((ext_vector_type(2)));
typedef float f32x4 __attribute__((ext_vector_type(4)));
typedef float f32x16 __attribute__((ext_vector_type(16)));
typedef unsigned u32x2 __attribute__((ext_vector_type(2)));
typedef unsigned u32x4 __attribute__((ext_vector_type(4)));
typedef __bf16 bf2_t __attribute__((ext_vector_type(2)));
typedef LAS char* lds_t;
typedef GAS unsigned gu32;
typedef __amdgpu_buffer_rsrc_t rsrc_t;

constexpr int SEQ = 2048, DM = 512;
#define PRIO_G 2
#define PRIO_C 0

constexpr unsigned WS_E    = 0;
constexpr unsigned WS_FU   = 1024;
constexpr unsigned WS_FKV  = 2048;
constexpr unsigned WS_FO   = 4096;
constexpr unsigned WS_FY   = 6144;
constexpr unsigned WS_G    = 8192;
constexpr unsigned WS_SLAB = 65536;
constexpr unsigned SLAB_B  = 4608;
constexpr unsigned WS_UX   = 2u << 20;
constexpr unsigned WS_YT   = 4u << 20;
constexpr unsigned WS_O    = 6u << 20;
constexpr unsigned WS_END  = 8u << 20;
constexpr size_t WS_STAMP = 16u << 20;

#define STAMP(k) do { } while (0)
#define PLACE() do { } while (0)

#define SB() __builtin_amdgcn_sched_barrier(0)
#define RLX_AGENT __ATOMIC_RELAXED, __HIP_MEMORY_SCOPE_AGENT
constexpr int AUX_SC1 = 16;

DI float bf2f(unsigned v) { return __uint_as_float(v << 16); }
DI float bflo(unsigned v) { return __uint_as_float(v << 16); }
DI float bfhi(unsigned v) { return __uint_as_float(v & 0xffff0000u); }
DI unsigned pk(float lo, float hi) { f32x2 v = {lo, hi}; return __builtin_bit_cast(unsigned, __builtin_convertvector(v, bf2_t)); }
DI unsigned pk2(f32x2 v) { return __builtin_bit_cast(unsigned, __builtin_convertvector(v, bf2_t)); }
DI float rbf(float f) { return bflo(pk(f, f)); }
DI float opq(float x) { asm("" : "+v"(x)); return x; }
DI bf16x8 as_frag(u32x4 v) { return __builtin_bit_cast(bf16x8, v); }
DI bf16x8 frag2(u32x2 a, u32x2 b) { u32x4 v = {a.x, a.y, b.x, b.y}; return as_frag(v); }
DI u32x4 lds_r128(lds_t p) { return *(LAS u32x4*)p; }
DI u32x2 lds_r64v(lds_t p) { return *(volatile LAS u32x2*)p; }
DI u32x2 lds_tr(lds_t p) { return __builtin_bit_cast(u32x2, __builtin_amdgcn_ds_read_tr16_b64_v4i16((LAS v4i16_t*)p)); }
DI void lds_w128(lds_t p, u32x4 v) { *(LAS u32x4*)p = v; }
DI void lds_w64(lds_t p, u32x2 v) { *(LAS u32x2*)p = v; }
DI void lds_w32(lds_t p, unsigned v) { *(LAS unsigned*)p = v; }
DI f32x16 mfma32(bf16x8 a, bf16x8 b, f32x16 c) { return __builtin_amdgcn_mfma_f32_32x32x16_bf16(a, b, c, 0, 0, 0); }
DI f32x4 mfma16(bf16x8 a, bf16x8 b, f32x4 c) { return __builtin_amdgcn_mfma_f32_16x16x32_bf16(a, b, c, 0, 0, 0); }
DI unsigned ldsaddr(lds_t p) { return (unsigned)(size_t)p; }
DI lds_t opaque(lds_t p) { unsigned a = ldsaddr(p); asm volatile("" : "+v"(a)); return (lds_t)(size_t)a; }
DI u32x4 ld_sc1(rsrc_t rs, unsigned off) { return __builtin_amdgcn_raw_buffer_load_b128(rs, (int)off, 0, AUX_SC1); }
DI void st_sc1(rsrc_t rs, unsigned off, u32x4 v) { __builtin_amdgcn_raw_buffer_store_b128(v, rs, (int)off, 0, AUX_SC1); }

DI void glds16(unsigned voff, const void* sbase, unsigned dst) {
    unsigned keep;
    asm volatile("s_mov_b32 %0, m0\n\ts_mov_b32 m0, %3\n\ts_nop 0\n\tglobal_load_lds_dwordx4 %1, %2\n\ts_mov_b32 m0, %0"
                 : "=&s"(keep) : "v"(voff), "s"(sbase), "s"(dst) : "memory");
}
DI void glds16_sc1(unsigned voff, const void* sbase, unsigned dst) {
    unsigned keep;
    asm volatile("s_mov_b32 %0, m0\n\ts_mov_b32 m0, %3\n\ts_nop 0\n\tglobal_load_lds_dwordx4 %1, %2 sc1\n\ts_mov_b32 m0, %0"
                 : "=&s"(keep) : "v"(voff), "s"(sbase), "s"(dst) : "memory");
}
#define WAIT_BAR(n) asm volatile("s_waitcnt vmcnt(%0) lgkmcnt(0)\n\ts_barrier" :: "n"(n) : "memory")
#define LDS_BAR() asm volatile("s_waitcnt lgkmcnt(0)\n\ts_barrier" ::: "memory")

DI void wait_flags(const char* ws, unsigned off, int n, unsigned tag, int lane) {
    const gu32* f = (const gu32*)(ws + off) + (lane & (n - 1));
#pragma nounroll
    for (unsigned spins = 0; spins < 200000u; ++spins) {
        const unsigned v = __hip_atomic_load(f, RLX_AGENT);
        if (__all(v == tag)) break;
        __builtin_amdgcn_s_sleep(1);
    }
    asm volatile("" ::: "memory");
}
DI void set_flag(char* ws, unsigned off, unsigned tag) { __hip_atomic_store((gu32*)(ws + off), tag, RLX_AGENT); }

constexpr float LOG2E = 1.4426950408889634f;

template <int CTRL> DI float dpp_f(float v) { return __int_as_float(__builtin_amdgcn_update_dpp(0, __float_as_int(v), CTRL, 0xf, 0xf, true)); }
DI float swap16_sum(float v) { auto r = __builtin_amdgcn_permlane16_swap(__float_as_uint(v), __float_as_uint(v), false, false); return opq(__uint_as_float(r[0]) + __uint_as_float(r[1])); }
DI float swap32_sum(float v) { auto r = __builtin_amdgcn_permlane32_swap(__float_as_uint(v), __float_as_uint(v), false, false); return opq(__uint_as_float(r[0]) + __uint_as_float(r[1])); }
DI float sum_stride4(float v) { v = opq(v + dpp_f<0x124>(v)); v = opq(v + dpp_f<0x128>(v)); return swap32_sum(swap16_sum(v)); }
DI float sum_wave(float v) { v += dpp_f<0x121>(v); v += dpp_f<0x122>(v); return sum_stride4(v); }

constexpr int P1_X = 0, P1_W = 32768, P1_WPHI = 69632;
constexpr int P1_PH = 0, P1_V = 57344;
constexpr int F_WO = 0, F_KVA = 65536, F_ZA = 67584, F_Y = 69632;

DI float max3f(float a, float b, float c) { return __builtin_elementwise_maximum(__builtin_elementwise_maximum(a, b), c); }
DI float min3f(float a, float b, float c) { return __builtin_elementwise_minimum(__builtin_elementwise_minimum(a, b), c); }
DI void softmax_pm(const f32x16& p, u32x4& plus, u32x4& minus) {
    float mx = __builtin_elementwise_maximum(max3f(p[0], p[1], p[2]), max3f(p[6], p[7], max3f(p[3], p[4], p[5])));
    float mn = __builtin_elementwise_minimum(min3f(p[0], p[1], p[2]), min3f(p[6], p[7], min3f(p[3], p[4], p[5])));
    {
        auto r = __builtin_amdgcn_permlane32_swap(__float_as_uint(mx), __float_as_uint(mx), false, false);
        mx = __builtin_elementwise_maximum(__uint_as_float(r[0]), __uint_as_float(r[1]));
        auto q = __builtin_amdgcn_permlane32_swap(__float_as_uint(mn), __float_as_uint(mn), false, false);
        mn = __builtin_elementwise_minimum(__uint_as_float(q[0]), __uint_as_float(q[1]));
    }
    const float cx = -LOG2E * mx, cn = LOG2E * mn;
    float ep[8], en[8];
#pragma unroll
    for (int i = 0; i < 8; ++i) {
        ep[i] = __builtin_amdgcn_exp2f(__builtin_fmaf(p[i], LOG2E, cx));
        en[i] = __builtin_amdgcn_exp2f(__builtin_fmaf(p[i], -LOG2E, cn));
    }
    float sp = opq(opq(opq(ep[0] + ep[1]) + opq(ep[2] + ep[3])) + opq(opq(ep[4] + ep[5]) + opq(ep[6] + ep[7])));
    float sn = opq(opq(opq(en[0] + en[1]) + opq(en[2] + en[3])) + opq(opq(en[4] + en[5]) + opq(en[6] + en[7])));
    {
        auto r = __builtin_amdgcn_permlane32_swap(__float_as_uint(sp), __float_as_uint(sp), false, false);
        sp = __uint_as_float(r[0]) + __uint_as_float(r[1]);
        auto q = __builtin_amdgcn_permlane32_swap(__float_as_uint(sn), __float_as_uint(sn), false, false);
        sn = __uint_as_float(q[0]) + __uint_as_float(q[1]);
    }
    const float rp = __builtin_amdgcn_rcpf(sp), rn = __builtin_amdgcn_rcpf(sn);
#pragma unroll
    for (int i = 0; i < 4; ++i) {
        plus[i]  = pk(opq(ep[2 * i] * rp), opq(ep[2 * i + 1] * rp));
        minus[i] = pk(opq(en[2 * i] * rn), opq(en[2 * i + 1] * rn));
    }
}

DI bf16x8 pack8(const f32x16& x, int s) {
    u32x4 v = {pk(x[8 * s], x[8 * s + 1]), pk(x[8 * s + 2], x[8 * s + 3]), pk(x[8 * s + 4], x[8 * s + 5]), pk(x[8 * s + 6], x[8 * s + 7])};
    return as_frag(v);
}

DI void g_role(const u16* __restrict__ x, const u16* __restrict__ wqkv, const u16* __restrict__ wphi, const u16* __restrict__ wo,
               const u16* __restrict__ dskip, char* __restrict__ ws, u16* __restrict__ out, int b, lds_t smem) {
    const int lane = threadIdx.x & 63;
    const int w = __builtin_amdgcn_readfirstlane(threadIdx.x >> 6);
    const int xcd = b & 7, i = b >> 3;
    const int rt = 4 * (xcd >> 1) + (i >> 3), hp = 8 * (xcd & 1) + (i & 7);
    const int c0 = ((i & 7) + 2 * (i >> 3)) & 7;
    const int t0 = 128 * rt;
    const rsrc_t rs = __builtin_amdgcn_make_buffer_rsrc((void*)ws, (short)0, (int)WS_END, 0x00020000);
    const unsigned sm = ldsaddr(smem);
    STAMP(0);
    __builtin_amdgcn_s_setprio(PRIO_G);

    unsigned xoff[4];
#pragma unroll
    for (int ii = 0; ii < 4; ++ii)
        xoff[ii] = (unsigned)((32 * w + 8 * ii + (lane >> 3)) * 1024 + (((lane & 7) ^ ((4 * ii + (lane >> 4)) & 7)) * 16));
    const unsigned woff = (unsigned)((16 * w + (lane >> 2)) * 3072 + (lane & 3) * 16);
    const char* xbase = (const char*)x + (size_t)t0 * 1024;
    const char* wbase = (const char*)wqkv + hp * 64;
    auto dma_x = [&](int c, int ii) { glds16(xoff[ii], xbase + ((c0 + c) & 7) * 128, sm + P1_X + (c & 1) * 16384 + (32 * w + 8 * ii) * 128); };
    auto dma_w = [&](int c, int j) { glds16(woff, wbase + (size_t)((c0 + c) & 7) * (64 * 3072) + j * 1024, sm + P1_W + (c % 3) * 12288 + j * 4096 + w * 1024); };
    if (w == 0) glds16((unsigned)(lane * 16), (const char*)wphi + hp * 1024, sm + P1_WPHI);
#pragma unroll
    for (int j = 0; j < 3; ++j) dma_w(0, j);
#pragma unroll
    for (int ii = 0; ii < 4; ++ii) dma_x(0, ii);
#pragma unroll
    for (int j = 0; j < 3; ++j) dma_w(1, j);
#pragma unroll
    for (int ii = 0; ii < 4; ++ii) dma_x(1, ii);
#pragma unroll
    for (int j = 0; j < 3; ++j) dma_w(2, j);
    const unsigned tag = __hip_atomic_load((const gu32*)(ws + WS_E), RLX_AGENT) + 1u;

    const int h = lane >> 5;
    const int kq = lane >> 4, q = (lane & 15) >> 2, p = lane & 3;
    const int trow = 32 * w + (lane & 31);
    lds_t xrd[4];
#pragma unroll
    for (int ks = 0; ks < 4; ++ks) xrd[ks] = opaque(smem + P1_X + trow * 128 + (((2 * ks + h) ^ ((trow >> 1) & 7)) * 16));
    const lds_t wrd = opaque(smem + P1_W + (8 * h + q) * 64 + 32 * (kq & 1) + 8 * p);
    f32x16 zero16;
#pragma unroll
    for (int r = 0; r < 16; ++r) zero16[r] = 0.f;
    f32x16 acc[3];
#pragma unroll
    for (int j = 0; j < 3; ++j) acc[j] = zero16;
    bf16x8 xf[2][4], wa[2][3];
    auto rd_x = [&](int c) {
#pragma unroll
        for (int ks = 0; ks < 4; ++ks) xf[c & 1][ks] = as_frag(lds_r128(xrd[ks] + (c & 1) * 16384));
    };
    auto rd_w = [&](int buf, int c, int ks, int j) {
        const lds_t pp = wrd + (c % 3) * 12288 + j * 4096 + ks * 1024;
        wa[buf][j] = frag2(lds_tr(pp), lds_tr(pp + 256));
    };
    WAIT_BAR(11);
    STAMP(1);
    rd_x(0); rd_w(0, 0, 0, 0); rd_w(0, 0, 0, 1); rd_w(0, 0, 0, 2);
    SB();
#define G_SLOT(c, ks, cur, NC, NKS, HAVE, D0, D1, D2)                                       \
    do {                                                                                    \
        if (HAVE) rd_w((cur) ^ 1, NC, NKS, 0);                                              \
        SB();                                                                               \
        acc[0] = mfma32(wa[cur][0], xf[(c) & 1][ks], acc[0]); D0;                           \
        SB();                                                                               \
        if (HAVE) rd_w((cur) ^ 1, NC, NKS, 1);                                              \
        SB();                                                                               \
        acc[1] = mfma32(wa[cur][1], xf[(c) & 1][ks], acc[1]); D1;                           \
        SB();                                                                               \
        if (HAVE) rd_w((cur) ^ 1, NC, NKS, 2);                                              \
        SB();                                                                               \
        acc[2] = mfma32(wa[cur][2], xf[(c) & 1][ks], acc[2]); D2;                           \
        SB();                                                                               \
    } while (0)
#pragma unroll
    for (int c = 0; c < 8; ++c) {
        const bool px = c + 2 < 8, pw = c + 3 < 8;
        G_SLOT(c, 0, 0, c, 1, true, if (px) dma_x(c + 2, 0), (void)0, if (px) dma_x(c + 2, 1));
        G_SLOT(c, 1, 1, c, 2, true, (void)0, if (px) dma_x(c + 2, 2), (void)0);
        G_SLOT(c, 2, 0, c, 3, true, if (px) dma_x(c + 2, 3), (void)0, (void)0);
        if (c < 7) {
            if (c <= 5) WAIT_BAR(7); else WAIT_BAR(0);
            rd_x(c + 1);
        }
        G_SLOT(c, 3, 1, c + 1, 0, c < 7, if (pw) dma_w(c + 3, 0), if (pw) dma_w(c + 3, 1), if (pw) dma_w(c + 3, 2));
    }
#undef G_SLOT
    STAMP(2);
    const lds_t wph = smem + P1_WPHI + (4 * h + q) * 32 + 8 * p;
    bf16x8 wf[2];
#pragma unroll
    for (int hh = 0; hh < 2; ++hh) wf[hh] = frag2(lds_tr(wph + hh * 512), lds_tr(wph + hh * 512 + 256));
    const unsigned sg = (unsigned)((trow >> 1) & 7);
    {
        const f32x16 p0 = mfma32(wf[0], pack8(acc[1], 0), zero16);
        const f32x16 p1 = mfma32(wf[1], pack8(acc[1], 1), zero16);
        const lds_t vrow = smem + P1_V + trow * 64;
#pragma unroll
        for (int hh = 0; hh < 2; ++hh)
#pragma unroll
            for (int g = 0; g < 2; ++g) {
                u32x2 v = {pk(acc[2][8 * hh + 4 * g], acc[2][8 * hh + 4 * g + 1]), pk(acc[2][8 * hh + 4 * g + 2], acc[2][8 * hh + 4 * g + 3])};
                lds_w64(vrow + (((4 * hh + 2 * g + h) ^ sg) * 8), v);
            }
#pragma unroll
        for (int hh = 0; hh < 2; ++hh) {
            u32x4 plus, minus;
            softmax_pm(hh == 0 ? p0 : p1, plus, minus);
            const lds_t row = smem + P1_PH + hh * 8192 + trow * 64;
#pragma unroll
            for (int ft = 0; ft < 2; ++ft) {
                lds_w64(row + (((4 * ft + h) ^ sg) * 8), (u32x2){plus[2 * ft], plus[2 * ft + 1]});
                lds_w64(row + (((4 * ft + 2 + h) ^ sg) * 8), (u32x2){minus[2 * ft], minus[2 * ft + 1]});
            }
        }
    }
    LDS_BAR();
    const int head = w >> 1, ft = w & 1;
    {
        f32x4 kv = {0.f, 0.f, 0.f, 0.f}, zz = {0.f, 0.f, 0.f, 0.f};
        const u32x4 onesv = {0x3F803F80u, 0x3F803F80u, 0x3F803F80u, 0x3F803F80u};
        const bf16x8 ones = as_frag(onesv);
        u32x2 a[4][2], bb[4][2];
#pragma unroll
        for (int ks = 0; ks < 4; ++ks)
#pragma unroll
            for (int tq = 0; tq < 2; ++tq) {
                const int t = 32 * ks + 8 * kq + 4 * tq + q;
                const int s = (t >> 1) & 7;
                a[ks][tq]  = lds_tr(smem + P1_PH + head * 8192 + t * 64 + (((4 * ft + p) ^ s) * 8));
                bb[ks][tq] = lds_tr(smem + P1_V + t * 64 + (((4 * head + p) ^ s) * 8));
            }
#pragma unroll
        for (int ks = 0; ks < 4; ++ks) {
            const bf16x8 af = frag2(a[ks][0], a[ks][1]);
            kv = mfma16(af, frag2(bb[ks][0], bb[ks][1]), kv);
            zz = mfma16(af, ones, zz);
        }
        const unsigned sb = WS_SLAB + (unsigned)(hp * 16 + rt) * SLAB_B;
        st_sc1(rs, sb + w * 1024 + lane * 16, __builtin_bit_cast(u32x4, kv));
        if ((lane & 15) == 0) st_sc1(rs, sb + 4096 + w * 64 + kq * 16, __builtin_bit_cast(u32x4, zz));
    }
    SB();
    const f32x16 pq0 = mfma32(wf[0], pack8(acc[0], 0), zero16);
    const f32x16 pq1 = mfma32(wf[1], pack8(acc[0], 1), zero16);
    SB();
    WAIT_BAR(0);
    if (threadIdx.x == 0) set_flag(ws, WS_FKV + hp * 128 + rt * 4, tag);
    STAMP(3);
    u32x4 qp[2], qm[2];
    softmax_pm(pq0, qp[0], qm[0]);
    softmax_pm(pq1, qp[1], qm[1]);
    SB();
    STAMP(4);
    if (w == 0) wait_flags(ws, WS_FKV + hp * 128, 16, tag, lane);
    asm volatile("s_barrier" ::: "memory");
    STAMP(5);
    {
        u32x4 sl[16];
        const unsigned sb = WS_SLAB + (unsigned)(hp * 16) * SLAB_B;
        const u32x4 zl = ld_sc1(rs, sb + (lane >> 2) * SLAB_B + 4096 + w * 64 + (lane & 3) * 16);
#pragma unroll
        for (int r = 0; r < 16; ++r) sl[r] = ld_sc1(rs, sb + r * SLAB_B + w * 1024 + lane * 16);
        float kv[4], z[4];
#pragma unroll
        for (int e = 0; e < 4; ++e) z[e] = sum_stride4(__uint_as_float(zl[e]));
#pragma unroll
        for (int e = 0; e < 4; ++e) kv[e] = __uint_as_float(sl[0][e]);
#pragma unroll
        for (int r = 1; r < 16; ++r)
#pragma unroll
            for (int e = 0; e < 4; ++e) kv[e] = opq(kv[e] + __uint_as_float(sl[r][e]));
        lds_w64(smem + F_KVA + ((((head * 2 + (kq >> 1)) * 2 + (kq & 1)) * 16 + (lane & 15)) * 2 + ft) * 8, (u32x2){pk(kv[0], kv[1]), pk(kv[2], kv[3])});
        if (lane < 4) lds_w64(smem + F_ZA + (((head * 2 + (lane >> 1)) * 2 + (lane & 1)) * 2 + ft) * 8, (u32x2){pk(z[0], z[1]), pk(z[2], z[3])});
    }
    LDS_BAR();
    STAMP(6);
    {
        bf16x8 af[2][2];
#pragma unroll
        for (int hh = 0; hh < 2; ++hh)
#pragma unroll
            for (int sn = 0; sn < 2; ++sn) {
                const int r = lane & 31;
                const lds_t pa = r < 16 ? smem + F_KVA + (((hh * 2 + sn) * 2 + h) * 16 + r) * 16 : smem + F_ZA + ((hh * 2 + sn) * 2 + h) * 16;
                af[hh][sn] = as_frag(lds_r128(pa));
            }
        f32x16 d0 = mfma32(af[0][0], as_frag(qp[0]), zero16);
        f32x16 d1 = mfma32(af[1][0], as_frag(qp[1]), zero16);
        d0 = mfma32(af[0][1], as_frag(qm[0]), d0);
        d1 = mfma32(af[1][1], as_frag(qm[1]), d1);
        const float r0 = __builtin_amdgcn_rcpf(d0[8] + 1e-6f), r1 = __builtin_amdgcn_rcpf(d1[8] + 1e-6f);
        const unsigned t16 = (unsigned)(t0 + trow) >> 4;
        const unsigned ob = WS_O + (t16 * 16 + hp) * 1024 + (h * 16 + (lane & 15)) * 16;
        st_sc1(rs, ob, (u32x4){pk(opq(d0[0] * r0), opq(d0[1] * r0)), pk(opq(d0[2] * r0), opq(d0[3] * r0)),
                               pk(opq(d1[0] * r1), opq(d1[1] * r1)), pk(opq(d1[2] * r1), opq(d1[3] * r1))});
        st_sc1(rs, ob + 512, (u32x4){pk(opq(d0[4] * r0), opq(d0[5] * r0)), pk(opq(d0[6] * r0), opq(d0[7] * r0)),
                                     pk(opq(d1[4] * r1), opq(d1[5] * r1)), pk(opq(d1[6] * r1), opq(d1[7] * r1))});
    }
    WAIT_BAR(0);
    if (threadIdx.x == 0) set_flag(ws, WS_FO + rt * 128 + hp * 4, tag);
    STAMP(7);
    const int rt64 = 2 * rt + (hp >> 3), ct = hp & 7;
    const int ft0 = 64 * rt64, n0 = 64 * ct;
#pragma unroll
    for (int ii = 0; ii < 16; ++ii) {
        const int k = 128 * w + 8 * ii + (lane >> 3);
        glds16((unsigned)(k * 1024 + (((lane & 7) ^ (((k >> 1) & 3) << 1)) * 16)), (const char*)wo + n0 * 2, sm + F_WO + (128 * w + 8 * ii) * 128);
    }
    const int tt = lane & 15;
    const int t = ft0 + 16 * w + tt;
    u32x2 uu[4], dk[4];
#pragma unroll
    for (int nt = 0; nt < 4; ++nt) {
        uu[nt] = *(const u32x2*)(x + (size_t)t * DM + n0 + 16 * nt + 4 * kq);
        dk[nt] = *(const u32x2*)(dskip + n0 + 16 * nt + 4 * kq);
    }
    if (w == 1) wait_flags(ws, WS_FY + ct * 128, 32, tag, lane);
    if (w == 2) wait_flags(ws, WS_FU + (rt64 >> 2) * 128 + (rt64 & 3) * 32, 8, tag, lane);
    if (w == 0) wait_flags(ws, WS_FO + (rt64 >> 1) * 128, 16, tag, lane);
    WAIT_BAR(0);
    STAMP(8);
    u32x4 of[16];
    {
        const unsigned ob = WS_O + (unsigned)(4 * rt64 + w) * 16384 + lane * 16;
#pragma unroll
        for (int k = 0; k < 16; ++k) of[k] = ld_sc1(rs, ob + k * 1024);
    }
#pragma unroll
    for (int ii = 0; ii < 2; ++ii) {
        const int dl = 16 * w + 8 * ii + (lane >> 3);
        glds16_sc1((unsigned)(dl * (SEQ * 2) + (((lane & 7) ^ (((dl >> 1) & 3) << 1)) * 16)), ws + WS_YT + ((size_t)n0 * SEQ + ft0) * 2,
                   sm + F_Y + (16 * w + 8 * ii) * 128);
    }
    const float g = __uint_as_float(__hip_atomic_load((const gu32*)(ws + WS_G + (t >> 3) * 128 + (t & 7) * 4), RLX_AGENT));
    f32x4 fa4[4];
#pragma unroll
    for (int nt = 0; nt < 4; ++nt) fa4[nt] = (f32x4){0.f, 0.f, 0.f, 0.f};
    {
        lds_t wp[4];
#pragma unroll
        for (int nt = 0; nt < 4; ++nt) {
            const int k = 4 * kq + q;
            wp[nt] = opaque(smem + F_WO + k * 128 + (((2 * nt + (p >> 1)) ^ (((k >> 1) & 3) << 1)) * 16) + 8 * (p & 1));
        }
        bf16x8 wa2[2][4];
#pragma unroll
        for (int nt = 0; nt < 4; ++nt) wa2[0][nt] = frag2(lds_tr(wp[nt]), lds_tr(wp[nt] + 2048));
#pragma unroll
        for (int k = 0; k < 16; ++k) {
            SB();
            if (k + 1 < 16) {
#pragma unroll
                for (int nt = 0; nt < 4; ++nt) wa2[(k + 1) & 1][nt] = frag2(lds_tr(wp[nt] + (k + 1) * 4096), lds_tr(wp[nt] + (k + 1) * 4096 + 2048));
            }
            SB();
#pragma unroll
            for (int nt = 0; nt < 4; ++nt) fa4[nt] = mfma16(wa2[k & 1][nt], as_frag(of[k]), fa4[nt]);
        }
    }
    STAMP(9);
    WAIT_BAR(0);
    u32x2 res[4];
    const float g1 = rbf(1.f - g);
#pragma unroll
    for (int nt = 0; nt < 4; ++nt) {
        const int dl = 16 * nt + 4 * kq + q;
        const int c = (16 * w + 4 * p) >> 3;
        const u32x2 yv = lds_tr(smem + F_Y + dl * 128 + ((c ^ (((dl >> 1) & 3) << 1)) * 16) + 8 * (p & 1));
        float o[4];
#pragma unroll
        for (int r2 = 0; r2 < 2; ++r2) {
            const unsigned us = pk(opq(bflo(uu[nt][r2]) * bflo(dk[nt][r2])), opq(bfhi(uu[nt][r2]) * bfhi(dk[nt][r2])));
            const unsigned a2 = pk(opq(bflo(yv[r2]) + bflo(us)), opq(bfhi(yv[r2]) + bfhi(us)));
            const unsigned b2 = pk(fa4[nt][2 * r2], fa4[nt][2 * r2 + 1]);
            const unsigned ga = pk(opq(g * bflo(a2)), opq(g * bfhi(a2)));
            const unsigned gb = pk(opq(g1 * bflo(b2)), opq(g1 * bfhi(b2)));
            o[2 * r2] = opq(bflo(ga) + bflo(gb));
            o[2 * r2 + 1] = opq(bfhi(ga) + bfhi(gb));
        }
        res[nt] = (u32x2){pk(o[0], o[1]), pk(o[2], o[3])};
    }
#pragma unroll
    for (int pr = 0; pr < 2; ++pr) {
        auto s0 = __builtin_amdgcn_permlane16_swap(res[2 * pr].x, res[2 * pr + 1].x, false, false);
        auto s1 = __builtin_amdgcn_permlane16_swap(res[2 * pr].y, res[2 * pr + 1].y, false, false);
        const int col = n0 + 32 * pr + (kq & 1) * 16 + (kq >> 1) * 8;
        *(u32x4*)(out + (size_t)t * DM + col) = (u32x4){s0[0], s1[0], s0[1], s1[1]};
    }
    STAMP(10);
}

constexpr int CV_XY = 0;
constexpr int CV_EX = 0;
constexpr int CV_KR = 8192;
constexpr int CV_Q  = 17408;
constexpr int CV_E2 = 0;
constexpr int CV_UT = 69632;
constexpr int QS = 4160;

DI f32x2 cexp_p(float a, float b, float p) {
    const float m = __builtin_amdgcn_exp2f(a * p * LOG2E);
    const float th = b * p;
    const float hi = th * 0.15915494309189535f;
    const float lo = __builtin_fmaf(th, 0.15915494309189535f, -hi) + th * 6.4206382149e-9f;
    const float fr = __builtin_amdgcn_fractf(hi) + lo;
    f32x2 r = {opq(m * __builtin_amdgcn_cosf(fr)), opq(m * __builtin_amdgcn_sinf(fr))};
    return r;
}
struct Cx { float x, y; };
DI Cx cmul(Cx a, Cx w) {
    Cx r;
    r.x = opq(__builtin_fmaf(-a.y, w.y, opq(a.x * w.x)));
    r.y = opq(__builtin_fmaf(a.y, w.x, opq(a.x * w.y)));
    return r;
}
DI unsigned pkc(Cx v) { return pk(v.x, v.y); }

struct CFrag { bf16x8 a[4][2], b[4]; };

DI void c_role(const u16* __restrict__ x, const u16* __restrict__ logA, const u16* __restrict__ Aim, const u16* __restrict__ Cre,
               const u16* __restrict__ Cim, const u16* __restrict__ logdt, const u16* __restrict__ gw, char* __restrict__ ws, int b, lds_t smem) {
    const int tid = threadIdx.x, lane = tid & 63;
    const int w = __builtin_amdgcn_readfirstlane(tid >> 6);
    const int ch = w >> 1, hf = w & 1;
    const int cg = b & 7, ci_ = b >> 3;
    const int cb = 32 * cg + ci_;
    const int d = 2 * cb + ch;
    const rsrc_t rs = __builtin_amdgcn_make_buffer_rsrc((void*)ws, (short)0, (int)WS_END, 0x00020000);
    STAMP(0);
    __builtin_amdgcn_s_setprio(PRIO_C);
    const unsigned tag = __hip_atomic_load((const gu32*)(ws + WS_E), RLX_AGENT) + 1u;

    u32x4 xt[2];
#pragma unroll
    for (int s = 0; s < 2; ++s) xt[s] = *(const u32x4*)(x + (size_t)(64 * ci_ + 32 * s + (tid >> 3)) * DM + 64 * cg + 8 * (tid & 7));
    const int n = 64 * hf + lane;
    const unsigned la = logA[d * 128 + n], ai = Aim[d * 128 + n], cr = Cre[d * 128 + n], ci = Cim[d * 128 + n], ld = logdt[d];
    u32x4 gx[2];
#pragma unroll
    for (int r = 0; r < 2; ++r) gx[r] = *(const u32x4*)(x + (size_t)(8 * cb + 2 * w + r) * DM + lane * 8);
    const u32x4 gwv = *(const u32x4*)(gw + lane * 8);
#pragma unroll
    for (int s = 0; s < 2; ++s) lds_w128(smem + CV_UT + (32 * s + (tid >> 3)) * 128 + (tid & 7) * 16, xt[s]);
    LDS_BAR();
    {
        const int g = lane >> 4, q = (lane & 15) >> 2, p = lane & 3;
#pragma unroll
        for (int s = 0; s < 2; ++s) {
            const lds_t a = smem + CV_UT + (8 * (g + 4 * s) + q) * 128 + 32 * w + 8 * p;
            const u32x2 lo = lds_tr(a), hi = lds_tr(a + 512);
            st_sc1(rs, WS_UX + (cg * 32 + ci_) * 8192 + (16 * w + (lane & 15)) * 128 + (g + 4 * s) * 16, (u32x4){lo.x, lo.y, hi.x, hi.y});
        }
    }
    {
        float gate[2];
#pragma unroll
        for (int r = 0; r < 2; ++r) {
            float s = 0.f;
#pragma unroll
            for (int j = 0; j < 4; ++j) {
                const unsigned pr = pk(opq(bflo(gx[r][j]) * bflo(gwv[j])), opq(bfhi(gx[r][j]) * bfhi(gwv[j])));
                s = opq(s + opq(bflo(pr) + bfhi(pr)));
            }
            s = sum_wave(s);
            gate[r] = rbf(__builtin_amdgcn_rcpf(1.f + __builtin_amdgcn_exp2f(-LOG2E * rbf(s))));
        }
        if (lane == 0)
            __hip_atomic_store((GAS unsigned long long*)(ws + WS_G + cb * 128 + w * 8),
                               ((unsigned long long)__float_as_uint(gate[1]) << 32) | __float_as_uint(gate[0]), RLX_AGENT);
    }
    STAMP(1);

    const lds_t tw = smem + CV_XY + w * 16384;
    {
        const float dt = rbf(expf(bf2f(ld)));
        const float a = -rbf(expf(bf2f(la))) * dt;
        const float bb = bf2f(ai) * dt;
        const f32x2 e1 = cexp_p(a, bb, 1.f), e8 = cexp_p(a, bb, 8.f), e256 = cexp_p(a, bb, 256.f);
        const lds_t row = tw + lane * 128;
        const int sw = lane & 7;
        auto put8 = [&](int tbl, int chunk, const Cx* v) {
            lds_w128(row + tbl * 8192 + ((chunk ^ sw) * 16), (u32x4){pk(v[0].x, v[1].x), pk(v[2].x, v[3].x), pk(v[4].x, v[5].x), pk(v[6].x, v[7].x)});
            lds_w128(row + tbl * 8192 + (((4 + chunk) ^ sw) * 16), (u32x4){pk(v[0].y, v[1].y), pk(v[2].y, v[3].y), pk(v[4].y, v[5].y), pk(v[6].y, v[7].y)});
        };
        {
            const Cx m8 = {e8.x, e8.y};
            Cx v[8];
            v[0] = (Cx){1.f, 0.f};
            v[1] = m8;
#pragma unroll
            for (int c = 2; c < 8; ++c) v[c] = cmul(v[c - 1], m8);
            put8(1, 0, v);
#pragma unroll
            for (int k = 1; k < 4; ++k) {
                v[0] = cmul(v[7], m8);
#pragma unroll
                for (int c = 1; c < 8; ++c) v[c] = cmul(v[c - 1], m8);
                put8(1, k, v);
            }
        }
        WAIT_BAR(0);
        if (tid == 0) set_flag(ws, WS_FU + cg * 128 + ci_ * 4, tag);
        {
            const Cx m1 = {e1.x, opq(-e1.y)}, m8 = {e8.x, opq(-e8.y)}, m256 = {e256.x, opq(-e256.y)};
            const Cx m2 = cmul(m1, m1);
            const Cx m4 = cmul(m2, m2);
            Cx v[8];
            v[0] = (Cx){opq(2.f * bf2f(cr) * dt), opq(-2.f * bf2f(ci) * dt)};
            v[1] = cmul(v[0], m1); v[2] = cmul(v[1], m1); v[3] = cmul(v[2], m1);
            v[4] = cmul(v[0], m256);
            v[5] = cmul(v[4], m2); v[6] = cmul(v[5], m2); v[7] = cmul(v[6], m2);
            put8(0, 0, v);
            Cx b256 = v[4];
            v[0] = cmul(v[3], m1); v[1] = cmul(v[0], m1); v[2] = cmul(v[1], m1); v[3] = cmul(v[2], m1);
            v[4] = cmul(v[7], m2);
            v[5] = cmul(b256, m256);
            v[6] = cmul(v[5], m4); v[7] = cmul(v[6], m4);
            put8(0, 1, v);
            v[4] = cmul(v[5], m256);
            v[5] = cmul(v[4], m4); v[6] = cmul(v[5], m4);
            v[7] = (Cx){0.f, 0.f};
            v[0] = cmul(v[4], m256); v[1] = cmul(v[0], m8);
            v[2] = cmul(v[0], m256); v[3] = cmul(v[2], m8);
            put8(0, 2, v);
            v[0] = cmul(v[2], m256); v[1] = cmul(v[0], m8);
            v[2] = cmul(v[0], m256); v[3] = cmul(v[2], m8);
            v[4] = v[5] = v[6] = v[7];
            put8(0, 3, v);
        }
    }
    STAMP(2);
    const int h = lane >> 5;
    f32x16 kd;
#pragma unroll
    for (int r = 0; r < 16; ++r) kd[r] = 0.f;
    {
        asm volatile("s_waitcnt lgkmcnt(0)" ::: "memory");
        const int blk = (lane >> 4) & 1, q = (lane & 15) >> 2, p = lane & 3;
        lds_t ra[2];
#pragma unroll
        for (int rho = 0; rho < 2; ++rho) {
            const int m = 4 * h + 2 * rho + (q >> 1);
            ra[rho] = opaque(tw + m * 128 + (((4 * (q & 1) + 2 * blk + (p >> 1)) ^ m) * 16) + 8 * (p & 1));
        }
#pragma unroll
        for (int s2 = 0; s2 < 2; ++s2) {
            bf16x8 fa[4], fb[4];
#pragma unroll
            for (int s = 0; s < 4; ++s) {
                fa[s] = frag2(lds_tr(ra[0] + (4 * s2 + s) * 1024), lds_tr(ra[1] + (4 * s2 + s) * 1024));
                fb[s] = frag2(lds_tr(ra[0] + 8192 + (4 * s2 + s) * 1024), lds_tr(ra[1] + 8192 + (4 * s2 + s) * 1024));
            }
#pragma unroll
            for (int s = 0; s < 4; ++s) kd = mfma32(fa[s], fb[s], kd);
        }
    }
    if (w == 0) wait_flags(ws, WS_FU + cg * 128, 32, tag, lane);
    LDS_BAR();
    STAMP(3);
    u32x4 ux[2];
#pragma unroll
    for (int s = 0; s < 2; ++s) ux[s] = ld_sc1(rs, WS_UX + (cg * 32 + 16 * s + (tid >> 4)) * 8192 + ci_ * 256 + (tid & 15) * 16);
    {
        const lds_t e = smem + CV_EX + w * 2048 + lane * 16;
        f32x4 s0, s1;
        if (hf == 0) { s0 = (f32x4){kd[8], kd[9], kd[10], kd[11]}; s1 = (f32x4){kd[12], kd[13], kd[14], kd[15]}; }
        else         { s0 = (f32x4){kd[0], kd[1], kd[2], kd[3]};   s1 = (f32x4){kd[4], kd[5], kd[6], kd[7]}; }
        lds_w128(e, __builtin_bit_cast(u32x4, s0));
        lds_w128(e + 1024, __builtin_bit_cast(u32x4, s1));
    }
    LDS_BAR();
    {
        const lds_t e = smem + CV_EX + (w ^ 1) * 2048 + lane * 16;
        const f32x4 s0 = __builtin_bit_cast(f32x4, lds_r128(e)), s1 = __builtin_bit_cast(f32x4, lds_r128(e + 1024));
        float k[8];
#pragma unroll
        for (int r = 0; r < 4; ++r) { k[r] = opq((hf == 0 ? kd[r] : kd[8 + r]) + s0[r]); k[4 + r] = opq((hf == 0 ? kd[4 + r] : kd[12 + r]) + s1[r]); }
        const int c = lane & 31;
        const lds_t kr = smem + CV_KR + ch * 4352 + 16;
        if (hf == 0) {
            u32x4 v;
            if (h == 0) v = (u32x4){pk(k[7], k[6]), pk(k[5], k[4]), pk(k[3], k[2]), pk(k[1], k[0])};
            else        v = (u32x4){pk(k[4], k[3]), pk(k[2], k[2]), pk(k[2], k[1]), pk(k[0], k[0])};
            lds_w128(kr + (32 * h + c) * 16, v);
            if (h == 1) { v = (u32x4){pk(k[7], k[7]), pk(k[6], k[6]), pk(k[6], k[5]), pk(k[5], k[5])}; lds_w128(kr + (64 + c) * 16, v); }
            if (lane == 0) { const u32x4 z = {0u, 0u, 0u, 0u}; lds_w128(kr - 16, z); }
        } else {
            if (h == 1) {
                const u32x4 v = {pk(k[2], k[2]), pk(k[1], k[1]), pk(k[1], k[0]), pk(k[0], k[0])};
                lds_w128(kr + (96 + c) * 16, v);
            } else {
#pragma unroll
                for (int al = 0; al < 4; ++al) {
                    const float k0 = k[2 * al], k8 = k[2 * al + 1];
                    const u32x4 v = {pk(k8, k8), pk(k8, (c & 1) ? k8 : k0), pk(k0, k0), pk(k0, k0)};
                    lds_w128(kr + (128 + 32 * al + c) * 16, v);
                }
            }
        }
    }
    LDS_BAR();
    {
        const lds_t qb = smem + CV_Q + ch * 16640;
#pragma unroll
        for (int it = 0; it < 2; ++it) {
            const int c8 = 128 * hf + 64 * it + lane;
            const lds_t kr = smem + CV_KR + ch * 4352 + c8 * 16;
            const u32x4 r = lds_r128(kr + 16);
            const u32x2 pv = *(LAS u32x2*)kr;
            const unsigned a0 = __builtin_amdgcn_alignbit(r[1], r[0], 16), a1 = __builtin_amdgcn_alignbit(r[2], r[1], 16),
                           a2 = __builtin_amdgcn_alignbit(r[3], r[2], 16), a3 = __builtin_amdgcn_alignbit(pv[0], r[3], 16),
                           a4 = __builtin_amdgcn_alignbit(pv[1], pv[0], 16);
            const lds_t q = qb + (2 * c8 + 8) * 8;
            lds_w128(q,          (u32x4){a3, a4, a1, a2});
            lds_w128(q + QS,     (u32x4){r[3], pv[0], r[1], r[2]});
            lds_w128(q + 2 * QS, (u32x4){a2, a3, a0, a1});
            lds_w128(q + 3 * QS, (u32x4){r[2], r[3], r[0], r[1]});
        }
        const u32x4 z = {0u, 0u, 0u, 0u};
        if (hf == 0 && lane < 16) lds_w128(qb + (lane >> 2) * QS + (lane & 3) * 16, z);
        if (hf == 1) lds_w128(smem + CV_UT + ch * 5120 + (lane >> 4) * 1280 + (lane & 15) * 16, z);
#pragma unroll
        for (int s = 0; s < 2; ++s) {
            const int rtile = 16 * s + (tid >> 4), piece = tid & 15, chunk = piece & 7;
            lds_w128(smem + CV_UT + (piece >> 3) * 5120 + (chunk & 3) * 1280 + (2 * rtile + (chunk >> 2) + 16) * 16, ux[s]);
        }
    }
    LDS_BAR();
    STAMP(4);
    f32x4 acc[4][2];
#pragma unroll
    for (int nn = 0; nn < 4; ++nn)
#pragma unroll
        for (int mb = 0; mb < 2; ++mb) acc[nn][mb] = (f32x4){0.f, 0.f, 0.f, 0.f};
    {
        const int r = lane & 15, kq = lane >> 4;
        const lds_t la0 = opaque(smem + CV_Q + ch * 16640 + (r & 3) * QS + ((r >> 2) - 2 * kq + 7) * 8 + hf * 8 * 64);
        const lds_t lb0 = opaque(smem + CV_UT + ch * 5120 + kq * 1280 + (r + 16) * 16 - hf * 8 * 16 - 7 * 16);
        auto rd_a = [&](CFrag& f, int dp, int m, int mb) {
            const lds_t p = la0 + dp * 64 + m * 1024 + mb * 32;
            const u32x2 f0 = lds_r64v(p + 8), f1 = lds_r64v(p);
            f.a[m][mb] = frag2(f0, f1);
        };
        auto rd_b = [&](CFrag& f, int dp, int mu) { f.b[mu] = as_frag(lds_r128(lb0 + (7 - dp) * 16 + mu * 256)); };
        CFrag fa, fb;
#pragma unroll
        for (int mu = 0; mu < 4; ++mu) rd_b(fa, 0, mu);
#pragma unroll
        for (int m = 0; m < 4; ++m) { rd_a(fa, 0, m, 0); rd_a(fa, 0, m, 1); }
        SB();
#define C_MM(cur, m, nn, mb) acc[nn][mb] = mfma16(cur.a[m][mb], cur.b[(nn) - (m)], acc[nn][mb])
#define C_STEP(cur, nxt, dp, HAVE)                                                                     \
        do {                                                                                           \
            if (HAVE) { rd_b(nxt, dp, 0); rd_b(nxt, dp, 1); }                                          \
            SB(); C_MM(cur, 0, 0, 0); C_MM(cur, 0, 0, 1); SB();                                        \
            if (HAVE) { rd_b(nxt, dp, 2); rd_b(nxt, dp, 3); }                                          \
            SB(); C_MM(cur, 0, 1, 0); C_MM(cur, 0, 1, 1); SB();                                        \
            if (HAVE) rd_a(nxt, dp, 0, 0);                                                             \
            SB(); C_MM(cur, 0, 2, 0); C_MM(cur, 0, 2, 1); SB();                                        \
            if (HAVE) rd_a(nxt, dp, 0, 1);                                                             \
            SB(); C_MM(cur, 0, 3, 0); C_MM(cur, 0, 3, 1); SB();                                        \
            if (HAVE) rd_a(nxt, dp, 1, 0);                                                             \
            SB(); C_MM(cur, 1, 1, 0); C_MM(cur, 1, 1, 1); SB();                                        \
            if (HAVE) rd_a(nxt, dp, 1, 1);                                                             \
            SB(); C_MM(cur, 1, 2, 0); C_MM(cur, 1, 2, 1); SB();                                        \
            if (HAVE) rd_a(nxt, dp, 2, 0);                                                             \
            SB(); C_MM(cur, 1, 3, 0); C_MM(cur, 1, 3, 1); SB();                                        \
            if (HAVE) rd_a(nxt, dp, 2, 1);                                                             \
            SB(); C_MM(cur, 2, 2, 0); C_MM(cur, 2, 2, 1); SB();                                        \
            if (HAVE) rd_a(nxt, dp, 3, 0);                                                             \
            SB(); C_MM(cur, 2, 3, 0); C_MM(cur, 2, 3, 1); SB();                                        \
            if (HAVE) rd_a(nxt, dp, 3, 1);                                                             \
            SB(); C_MM(cur, 3, 3, 0); C_MM(cur, 3, 3, 1); SB();                                        \
        } while (0)
#pragma unroll
        for (int dp = 0; dp < 8; dp += 2) {
            C_STEP(fa, fb, dp + 1, true);
            C_STEP(fb, fa, dp + 2, dp + 2 < 8);
        }
#undef C_STEP
#undef C_MM
    }
    STAMP(5);
    {
        f32x4 mine[2][2], other[2][2];
        if (hf == 0) {
#pragma unroll
            for (int q = 0; q < 2; ++q)
#pragma unroll
                for (int mb = 0; mb < 2; ++mb) { mine[q][mb] = acc[q][mb]; other[q][mb] = acc[2 + q][mb]; }
        } else {
#pragma unroll
            for (int q = 0; q < 2; ++q)
#pragma unroll
                for (int mb = 0; mb < 2; ++mb) { mine[q][mb] = acc[2 + q][mb]; other[q][mb] = acc[q][mb]; }
        }
        const lds_t e = smem + CV_E2 + w * 4096 + lane * 16;
#pragma unroll
        for (int q = 0; q < 2; ++q)
#pragma unroll
            for (int mb = 0; mb < 2; ++mb) lds_w128(e + (2 * q + mb) * 1024, __builtin_bit_cast(u32x4, other[q][mb]));
        LDS_BAR();
        const lds_t e2 = smem + CV_E2 + (w ^ 1) * 4096 + lane * 16;
        const int kq = lane >> 4;
#pragma unroll
        for (int q = 0; q < 2; ++q) {
            u32x2 pm[2];
#pragma unroll
            for (int mb = 0; mb < 2; ++mb) {
                const f32x4 o = __builtin_bit_cast(f32x4, lds_r128(e2 + (2 * q + mb) * 1024));
                const f32x4 m4 = mine[q][mb];
                pm[mb] = (u32x2){pk(opq(m4[0] + o[0]), opq(m4[1] + o[1])), pk(opq(m4[2] + o[2]), opq(m4[3] + o[3]))};
            }
            auto s0 = __builtin_amdgcn_permlane16_swap(pm[0].x, pm[1].x, false, false);
            auto s1 = __builtin_amdgcn_permlane16_swap(pm[0].y, pm[1].y, false, false);
            const int t = 512 * (2 * hf + q) + 32 * (lane & 15) + (kq & 1) * 16 + (kq >> 1) * 8;
            st_sc1(rs, WS_YT + (unsigned)(d * SEQ + t) * 2, (u32x4){s0[0], s1[0], s0[1], s1[1]});
        }
    }
    WAIT_BAR(0);
    if (tid == 0) set_flag(ws, WS_FY + cg * 128 + ci_ * 4, tag);
    STAMP(6);
    if (b == 0 && w == 0) {
#pragma nounroll
        for (unsigned spins = 0; spins < 200000u; ++spins) {
            const u32x4 fu = ld_sc1(rs, WS_FU + lane * 16);
            const u32x4 fk = ld_sc1(rs, WS_FKV + (lane >> 2) * 128 + (lane & 3) * 16);
            const bool ok = fu.x == tag && fu.y == tag && fu.z == tag && fu.w == tag && fk.x == tag && fk.y == tag && fk.z == tag && fk.w == tag;
            if (__all(ok)) break;
            __builtin_amdgcn_s_sleep(1);
        }
        if (lane == 0) set_flag(ws, WS_E, tag);
    }
}

__global__ __launch_bounds__(256, 2) void fused_mix(const u16* __restrict__ x, const u16* __restrict__ logA, const u16* __restrict__ Aim,
                                                    const u16* __restrict__ Cre, const u16* __restrict__ Cim, const u16* __restrict__ logdt,
                                                    const u16* __restrict__ dskip, const u16* __restrict__ wqkv, const u16* __restrict__ wphi,
                                                    const u16* __restrict__ wo, const u16* __restrict__ gw, char* __restrict__ ws,
                                                    u16* __restrict__ out) {
    __shared__ __attribute__((aligned(16))) char smem_[79872];
    const lds_t smem = (lds_t)smem_;
    const int b = blockIdx.x;
    PLACE();
    if (b < 256) g_role(x, wqkv, wphi, wo, dskip, ws, out, b, smem);
    if (b >= 256) c_role(x, logA, Aim, Cre, Cim, logdt, gw, ws, b - 256, smem);
}

extern "C" void kernel_launch(void* const* d_in, const int* in_sizes, int n_in, void* d_out, int out_size, void* d_ws, size_t ws_size,
                              hipStream_t stream) {
    fused_mix<<<512, 256, 0, stream>>>((const u16*)d_in[0], (const u16*)d_in[1], (const u16*)d_in[2], (const u16*)d_in[3], (const u16*)d_in[4],
                                       (const u16*)d_in[5], (const u16*)d_in[6], (const u16*)d_in[8], (const u16*)d_in[9], (const u16*)d_in[10],
                                       (const u16*)d_in[11], (char*)d_ws, (u16*)d_out);
}
```

```cpp
#include <hip/hip_runtime.h>
#include <stdint.h>

#define DI __device__ __forceinline__
#define LAS __attribute__((address_space(3)))
#define GAS __attribute__((address_space(1)))
typedef unsigned short u16;
typedef short bf16x8 __attribute__((ext_vector_type(8)));
typedef short v4i16_t __attribute__((ext_vector_type(4)));
typedef float f32x2 __attribute__((ext_vector_type(2)));
typedef float f32x4 __attribute__((ext_vector_type(4)));
typedef float f32x16 __attribute__((ext_vector_type(16)));
typedef unsigned u32x2 __attribute__((ext_vector_type(2)));
typedef unsigned u32x4 __attribute__((ext_vector_type(4)));
typedef __bf16 bf2_t __attribute__((ext_vector_type(2)));
typedef LAS char* lds_t;
typedef GAS unsigned gu32;
typedef __amdgpu_buffer_rsrc_t rsrc_t;

constexpr int SEQ = 2048, DM = 512;
constexpr unsigned SPIN_CAP = 200000u;

constexpr unsigned WS_E    = 0;
constexpr unsigned WS_FU   = 1024;
constexpr unsigned WS_FKV  = 2048;
constexpr unsigned WS_FO   = 4096;
constexpr unsigned WS_FY   = 6144;
constexpr unsigned WS_G    = 8192;
constexpr unsigned WS_SLAB = 65536;
constexpr unsigned SLAB_B  = 4608;
constexpr unsigned WS_UX   = 2u << 20;
constexpr unsigned WS_YT   = 4u << 20;
constexpr unsigned WS_O    = 6u << 20;
constexpr unsigned WS_END  = 8u << 20;

#define SB() __builtin_amdgcn_sched_barrier(0)
#define RLX_AGENT __ATOMIC_RELAXED, __HIP_MEMORY_SCOPE_AGENT
constexpr int AUX_SC1 = 16;

DI float bf2f(unsigned v) { return __uint_as_float(v << 16); }
DI float bflo(unsigned v) { return __uint_as_float(v << 16); }
DI float bfhi(unsigned v) { return __uint_as_float(v & 0xffff0000u); }
DI unsigned pk(float lo, float hi) { f32x2 v = {lo, hi}; return __builtin_bit_cast(unsigned, __builtin_convertvector(v, bf2_t)); }
DI float rbf(float f) { return bflo(pk(f, f)); }
DI float opq(float x) { asm("" : "+v"(x)); return x; }
DI bf16x8 as_frag(u32x4 v) { return __builtin_bit_cast(bf16x8, v); }
DI bf16x8 frag2(u32x2 a, u32x2 b) { u32x4 v = {a.x, a.y, b.x, b.y}; return as_frag(v); }
DI u32x4 lds_r128(lds_t p) { return *(LAS u32x4*)p; }
DI u32x2 lds_r64v(lds_t p) { return *(volatile LAS u32x2*)p; }
DI u32x2 lds_tr(lds_t p) { return __builtin_bit_cast(u32x2, __builtin_amdgcn_ds_read_tr16_b64_v4i16((LAS v4i16_t*)p)); }
DI void lds_w128(lds_t p, u32x4 v) { *(LAS u32x4*)p = v; }
DI void lds_w64(lds_t p, u32x2 v) { *(LAS u32x2*)p = v; }
DI f32x16 mfma32(bf16x8 a, bf16x8 b, f32x16 c) { return __builtin_amdgcn_mfma_f32_32x32x16_bf16(a, b, c, 0, 0, 0); }
DI f32x4 mfma16(bf16x8 a, bf16x8 b, f32x4 c) { return __builtin_amdgcn_mfma_f32_16x16x32_bf16(a, b, c, 0, 0, 0); }
DI unsigned ldsaddr(lds_t p) { return (unsigned)(size_t)p; }
DI lds_t opaque(lds_t p) { unsigned a = ldsaddr(p); asm volatile("" : "+v"(a)); return (lds_t)(size_t)a; }
DI u32x4 ld_sc1(rsrc_t rs, unsigned off) { return __builtin_amdgcn_raw_buffer_load_b128(rs, (int)off, 0, AUX_SC1); }
DI void st_sc1(rsrc_t rs, unsigned off, u32x4 v) { __builtin_amdgcn_raw_buffer_store_b128(v, rs, (int)off, 0, AUX_SC1); }

DI void glds16(unsigned voff, const void* sbase, unsigned dst) {
    unsigned keep;
    asm volatile("s_mov_b32 %0, m0\n\ts_mov_b32 m0, %3\n\ts_nop 0\n\tglobal_load_lds_dwordx4 %1, %2\n\ts_mov_b32 m0, %0"
                 : "=&s"(keep) : "v"(voff), "s"(sbase), "s"(dst) : "memory");
}
DI void glds16_sc1(unsigned voff, const void* sbase, unsigned dst) {
    unsigned keep;
    asm volatile("s_mov_b32 %0, m0\n\ts_mov_b32 m0, %3\n\ts_nop 0\n\tglobal_load_lds_dwordx4 %1, %2 sc1\n\ts_mov_b32 m0, %0"
                 : "=&s"(keep) : "v"(voff), "s"(sbase), "s"(dst) : "memory");
}
DI void touch_lines(const char* p, unsigned dst) {
    unsigned keep;
    asm volatile("s_mov_b32 %0, m0\n\ts_mov_b32 m0, %2\n\ts_nop 0\n\tglobal_load_lds_dword %1, off\n\ts_mov_b32 m0, %0"
                 : "=&s"(keep) : "v"(p), "s"(dst) : "memory");
}
#define WAIT_BAR(n) asm volatile("s_waitcnt vmcnt(%0) lgkmcnt(0)\n\ts_barrier" :: "n"(n) : "memory")
#define LDS_BAR() asm volatile("s_waitcnt lgkmcnt(0)\n\ts_barrier" ::: "memory")

DI void wait_flags(const char* ws, unsigned off, int n, unsigned tag, int lane) {
    const gu32* f = (const gu32*)(ws + off) + (lane & (n - 1));
#pragma nounroll
    for (unsigned spins = 0; spins < SPIN_CAP; ++spins) {
        const unsigned v = __hip_atomic_load(f, RLX_AGENT);
        if (__all(v == tag)) break;
        __builtin_amdgcn_s_sleep(1);
    }
    asm volatile("" ::: "memory");
}
DI void set_flag(char* ws, unsigned off, unsigned tag) { __hip_atomic_store((gu32*)(ws + off), tag, RLX_AGENT); }

constexpr float LOG2E = 1.4426950408889634f;

template <int CTRL> DI float dpp_f(float v) { return __int_as_float(__builtin_amdgcn_update_dpp(0, __float_as_int(v), CTRL, 0xf, 0xf, true)); }
DI float swap16_sum(float v) { auto r = __builtin_amdgcn_permlane16_swap(__float_as_uint(v), __float_as_uint(v), false, false); return opq(__uint_as_float(r[0]) + __uint_as_float(r[1])); }
DI float swap32_sum(float v) { auto r = __builtin_amdgcn_permlane32_swap(__float_as_uint(v), __float_as_uint(v), false, false); return opq(__uint_as_float(r[0]) + __uint_as_float(r[1])); }
DI float sum_stride4(float v) { v = opq(v + dpp_f<0x124>(v)); v = opq(v + dpp_f<0x128>(v)); return swap32_sum(swap16_sum(v)); }
DI float sum_wave(float v) { v += dpp_f<0x121>(v); v += dpp_f<0x122>(v); return sum_stride4(v); }

constexpr int P1_X = 0, P1_W = 32768, P1_WPHI = 69632;
constexpr int P1_PH = 0, P1_V = 57344;
constexpr int F_WO = 0, F_KVA = 65536, F_ZA = 67584, F_Y = 69632;
constexpr int OFD = 5;

DI float max3f(float a, float b, float c) { return __builtin_elementwise_maximum(__builtin_elementwise_maximum(a, b), c); }
DI float min3f(float a, float b, float c) { return __builtin_elementwise_minimum(__builtin_elementwise_minimum(a, b), c); }
DI void softmax_pm(const f32x16& p, u32x4& plus, u32x4& minus) {
    float mx = __builtin_elementwise_maximum(max3f(p[0], p[1], p[2]), max3f(p[6], p[7], max3f(p[3], p[4], p[5])));
    float mn = __builtin_elementwise_minimum(min3f(p[0], p[1], p[2]), min3f(p[6], p[7], min3f(p[3], p[4], p[5])));
    {
        auto r = __builtin_amdgcn_permlane32_swap(__float_as_uint(mx), __float_as_uint(mx), false, false);
        mx = __builtin_elementwise_maximum(__uint_as_float(r[0]), __uint_as_float(r[1]));
        auto q = __builtin_amdgcn_permlane32_swap(__float_as_uint(mn), __float_as_uint(mn), false, false);
        mn = __builtin_elementwise_minimum(__uint_as_float(q[0]), __uint_as_float(q[1]));
    }
    const float cx = -LOG2E * mx, cn = LOG2E * mn;
    float ep[8], en[8];
#pragma unroll
    for (int i = 0; i < 8; ++i) {
        ep[i] = __builtin_amdgcn_exp2f(__builtin_fmaf(p[i], LOG2E, cx));
        en[i] = __builtin_amdgcn_exp2f(__builtin_fmaf(p[i], -LOG2E, cn));
    }
    float sp = opq(opq(opq(ep[0] + ep[1]) + opq(ep[2] + ep[3])) + opq(opq(ep[4] + ep[5]) + opq(ep[6] + ep[7])));
    float sn = opq(opq(opq(en[0] + en[1]) + opq(en[2] + en[3])) + opq(opq(en[4] + en[5]) + opq(en[6] + en[7])));
    {
        auto r = __builtin_amdgcn_permlane32_swap(__float_as_uint(sp), __float_as_uint(sp), false, false);
        sp = __uint_as_float(r[0]) + __uint_as_float(r[1]);
        auto q = __builtin_amdgcn_permlane32_swap(__float_as_uint(sn), __float_as_uint(sn), false, false);
        sn = __uint_as_float(q[0]) + __uint_as_float(q[1]);
    }
    const float rp = __builtin_amdgcn_rcpf(sp), rn = __builtin_amdgcn_rcpf(sn);
#pragma unroll
    for (int i = 0; i < 4; ++i) {
        plus[i]  = pk(opq(ep[2 * i] * rp), opq(ep[2 * i + 1] * rp));
        minus[i] = pk(opq(en[2 * i] * rn), opq(en[2 * i + 1] * rn));
    }
}

DI bf16x8 pack8(const f32x16& x, int s) {
    u32x4 v = {pk(x[8 * s], x[8 * s + 1]), pk(x[8 * s + 2], x[8 * s + 3]), pk(x[8 * s + 4], x[8 * s + 5]), pk(x[8 * s + 6], x[8 * s + 7])};
    return as_frag(v);
}

DI void g_role(const u16* __restrict__ x, const u16* __restrict__ wqkv, const u16* __restrict__ wphi, const u16* __restrict__ wo,
               const u16* __restrict__ dskip, char* __restrict__ ws, u16* __restrict__ out, int b, lds_t smem) {
    const int lane = threadIdx.x & 63;
    const int w = __builtin_amdgcn_readfirstlane(threadIdx.x >> 6);
    const int xcd = b & 7, i = b >> 3;
    const int rt = 4 * (xcd >> 1) + (i >> 3), hp = 8 * (xcd & 1) + (i & 7);
    const int c0 = ((i & 7) + 2 * (i >> 3)) & 7;
    const int t0 = 128 * rt;
    const rsrc_t rs = __builtin_amdgcn_make_buffer_rsrc((void*)ws, (short)0, (int)WS_END, 0x00020000);
    const unsigned sm = ldsaddr(smem);
    __builtin_amdgcn_s_setprio(2);

    unsigned xoff[4];
#pragma unroll
    for (int ii = 0; ii < 4; ++ii)
        xoff[ii] = (unsigned)((32 * w + 8 * ii + (lane >> 3)) * 1024 + (((lane & 7) ^ ((4 * ii + (lane >> 4)) & 7)) * 16));
    const unsigned woff = (unsigned)((16 * w + (lane >> 2)) * 3072 + (lane & 3) * 16);
    const char* xbase = (const char*)x + (size_t)t0 * 1024;
    const char* wbase = (const char*)wqkv + hp * 64;
    auto dma_x = [&](int c, int ii) { glds16(xoff[ii], xbase + ((c0 + c) & 7) * 128, sm + P1_X + (c & 1) * 16384 + (32 * w + 8 * ii) * 128); };
    auto dma_w = [&](int c, int j) { glds16(woff, wbase + (size_t)((c0 + c) & 7) * (64 * 3072) + j * 1024, sm + P1_W + (c % 3) * 12288 + j * 4096 + w * 1024); };
    if (w == 0) glds16((unsigned)(lane * 16), (const char*)wphi + hp * 1024, sm + P1_WPHI);
#pragma unroll
    for (int j = 0; j < 3; ++j) dma_w(0, j);
#pragma unroll
    for (int ii = 0; ii < 4; ++ii) dma_x(0, ii);
#pragma unroll
    for (int j = 0; j < 3; ++j) dma_w(1, j);
#pragma unroll
    for (int ii = 0; ii < 4; ++ii) dma_x(1, ii);
#pragma unroll
    for (int j = 0; j < 3; ++j) dma_w(2, j);
    const unsigned tag = __hip_atomic_load((const gu32*)(ws + WS_E), RLX_AGENT) + 1u;

    const int h = lane >> 5;
    const int kq = lane >> 4, q = (lane & 15) >> 2, p = lane & 3;
    const int trow = 32 * w + (lane & 31);
    lds_t xrd[4];
#pragma unroll
    for (int ks = 0; ks < 4; ++ks) xrd[ks] = opaque(smem + P1_X + trow * 128 + (((2 * ks + h) ^ ((trow >> 1) & 7)) * 16));
    const lds_t wrd = opaque(smem + P1_W + (8 * h + q) * 64 + 32 * (kq & 1) + 8 * p);
    f32x16 zero16;
#pragma unroll
    for (int r = 0; r < 16; ++r) zero16[r] = 0.f;
    f32x16 acc[3];
#pragma unroll
    for (int j = 0; j < 3; ++j) acc[j] = zero16;
    bf16x8 xf[2][4], wa[2][3];
    auto rd_x = [&](int c) {
#pragma unroll
        for (int ks = 0; ks < 4; ++ks) xf[c & 1][ks] = as_frag(lds_r128(xrd[ks] + (c & 1) * 16384));
    };
    auto rd_w = [&](int buf, int c, int ks, int j) {
        const lds_t pp = wrd + (c % 3) * 12288 + j * 4096 + ks * 1024;
        wa[buf][j] = frag2(lds_tr(pp), lds_tr(pp + 256));
    };
    WAIT_BAR(10);
    rd_x(0); rd_w(0, 0, 0, 0); rd_w(0, 0, 0, 1); rd_w(0, 0, 0, 2);
    SB();
#define G_SLOT(c, ks, cur, NC, NKS, HAVE, D0, D1, D2)                                       \
    do {                                                                                    \
        if (HAVE) rd_w((cur) ^ 1, NC, NKS, 0);                                              \
        SB();                                                                               \
        acc[0] = mfma32(wa[cur][0], xf[(c) & 1][ks], acc[0]); D0;                           \
        SB();                                                                               \
        if (HAVE) rd_w((cur) ^ 1, NC, NKS, 1);                                              \
        SB();                                                                               \
        acc[1] = mfma32(wa[cur][1], xf[(c) & 1][ks], acc[1]); D1;                           \
        SB();                                                                               \
        if (HAVE) rd_w((cur) ^ 1, NC, NKS, 2);                                              \
        SB();                                                                               \
        acc[2] = mfma32(wa[cur][2], xf[(c) & 1][ks], acc[2]); D2;                           \
        SB();                                                                               \
    } while (0)
#pragma unroll
    for (int c = 0; c < 8; ++c) {
        const bool px = c + 2 < 8, pw = c + 3 < 8;
        G_SLOT(c, 0, 0, c, 1, true, if (px) dma_x(c + 2, 0), (void)0, if (px) dma_x(c + 2, 1));
        G_SLOT(c, 1, 1, c, 2, true, (void)0, if (px) dma_x(c + 2, 2), (void)0);
        G_SLOT(c, 2, 0, c, 3, true, if (px) dma_x(c + 2, 3), (void)0, (void)0);
        if (c < 7) {
            if (c <= 5) WAIT_BAR(7); else WAIT_BAR(0);
            rd_x(c + 1);
        }
        G_SLOT(c, 3, 1, c + 1, 0, c < 7, if (pw) dma_w(c + 3, 0), if (pw) dma_w(c + 3, 1), if (pw) dma_w(c + 3, 2));
    }
#undef G_SLOT
    const lds_t wph = smem + P1_WPHI + (4 * h + q) * 32 + 8 * p;
    bf16x8 wf[2];
#pragma unroll
    for (int hh = 0; hh < 2; ++hh) wf[hh] = frag2(lds_tr(wph + hh * 512), lds_tr(wph + hh * 512 + 256));
    const unsigned sg = (unsigned)((trow >> 1) & 7);
    {
        const f32x16 p0 = mfma32(wf[0], pack8(acc[1], 0), zero16);
        const f32x16 p1 = mfma32(wf[1], pack8(acc[1], 1), zero16);
        const lds_t vrow = smem + P1_V + trow * 64;
#pragma unroll
        for (int hh = 0; hh < 2; ++hh)
#pragma unroll
            for (int g = 0; g < 2; ++g) {
                u32x2 v = {pk(acc[2][8 * hh + 4 * g], acc[2][8 * hh + 4 * g + 1]), pk(acc[2][8 * hh + 4 * g + 2], acc[2][8 * hh + 4 * g + 3])};
                lds_w64(vrow + (((4 * hh + 2 * g + h) ^ sg) * 8), v);
            }
#pragma unroll
        for (int hh = 0; hh < 2; ++hh) {
            u32x4 plus, minus;
            softmax_pm(hh == 0 ? p0 : p1, plus, minus);
            const lds_t row = smem + P1_PH + hh * 8192 + trow * 64;
#pragma unroll
            for (int ft = 0; ft < 2; ++ft) {
                lds_w64(row + (((4 * ft + h) ^ sg) * 8), (u32x2){plus[2 * ft], plus[2 * ft + 1]});
                lds_w64(row + (((4 * ft + 2 + h) ^ sg) * 8), (u32x2){minus[2 * ft], minus[2 * ft + 1]});
            }
        }
    }
    LDS_BAR();
    const int head = w >> 1, ft = w & 1;
    {
        f32x4 kv = {0.f, 0.f, 0.f, 0.f}, zz = {0.f, 0.f, 0.f, 0.f};
        const u32x4 onesv = {0x3F803F80u, 0x3F803F80u, 0x3F803F80u, 0x3F803F80u};
        const bf16x8 ones = as_frag(onesv);
        u32x2 a[4][2], bb[4][2];
#pragma unroll
        for (int ks = 0; ks < 4; ++ks)
#pragma unroll
            for (int tq = 0; tq < 2; ++tq) {
                const int t = 32 * ks + 8 * kq + 4 * tq + q;
                const int s = (t >> 1) & 7;
                a[ks][tq]  = lds_tr(smem + P1_PH + head * 8192 + t * 64 + (((4 * ft + p) ^ s) * 8));
                bb[ks][tq] = lds_tr(smem + P1_V + t * 64 + (((4 * head + p) ^ s) * 8));
            }
#pragma unroll
        for (int ks = 0; ks < 4; ++ks) {
            const bf16x8 af = frag2(a[ks][0], a[ks][1]);
            kv = mfma16(af, frag2(bb[ks][0], bb[ks][1]), kv);
            zz = mfma16(af, ones, zz);
        }
        const unsigned sb = WS_SLAB + (unsigned)(hp * 16 + rt) * SLAB_B;
        st_sc1(rs, sb + w * 1024 + lane * 16, __builtin_bit_cast(u32x4, kv));
        if ((lane & 15) == 0) st_sc1(rs, sb + 4096 + w * 64 + kq * 16, __builtin_bit_cast(u32x4, zz));
    }
    SB();
    const f32x16 pq0 = mfma32(wf[0], pack8(acc[0], 0), zero16);
    const f32x16 pq1 = mfma32(wf[1], pack8(acc[0], 1), zero16);
    SB();
    WAIT_BAR(0);
    if (threadIdx.x == 0) set_flag(ws, WS_FKV + hp * 128 + rt * 4, tag);
    const int rt64 = 2 * rt + (hp >> 3), ct = hp & 7;
    const int ft0 = 64 * rt64, n0 = 64 * ct;
    const int t = ft0 + 16 * w + (lane & 15);
    u32x2 uu[4], dk[4];
    u32x4 qp[2], qm[2];
    softmax_pm(pq0, qp[0], qm[0]);
    softmax_pm(pq1, qp[1], qm[1]);
    SB();
    if (w == 0) wait_flags(ws, WS_FKV + hp * 128, 16, tag, lane);
    asm volatile("s_barrier" ::: "memory");
    {
        u32x4 sl[16];
        const unsigned sb = WS_SLAB + (unsigned)(hp * 16) * SLAB_B;
        const u32x4 zl = ld_sc1(rs, sb + (lane >> 2) * SLAB_B + 4096 + w * 64 + (lane & 3) * 16);
#pragma unroll
        for (int r = 0; r < 16; ++r) sl[r] = ld_sc1(rs, sb + r * SLAB_B + w * 1024 + lane * 16);
        SB();
#pragma unroll
        for (int nt = 0; nt < 4; ++nt) {
            uu[nt] = *(const u32x2*)(x + (size_t)t * DM + n0 + 16 * nt + 4 * kq);
            dk[nt] = *(const u32x2*)(dskip + n0 + 16 * nt + 4 * kq);
        }
        SB();
        float kv[4], z[4];
#pragma unroll
        for (int e = 0; e < 4; ++e) z[e] = sum_stride4(__uint_as_float(zl[e]));
#pragma unroll
        for (int e = 0; e < 4; ++e) kv[e] = __uint_as_float(sl[0][e]);
#pragma unroll
        for (int r = 1; r < 16; ++r)
#pragma unroll
            for (int e = 0; e < 4; ++e) kv[e] = opq(kv[e] + __uint_as_float(sl[r][e]));
        lds_w64(smem + F_KVA + ((((head * 2 + (kq >> 1)) * 2 + (kq & 1)) * 16 + (lane & 15)) * 2 + ft) * 8, (u32x2){pk(kv[0], kv[1]), pk(kv[2], kv[3])});
        if (lane < 4) lds_w64(smem + F_ZA + (((head * 2 + (lane >> 1)) * 2 + (lane & 1)) * 2 + ft) * 8, (u32x2){pk(z[0], z[1]), pk(z[2], z[3])});
    }
    LDS_BAR();
    {
        bf16x8 af[2][2];
#pragma unroll
        for (int hh = 0; hh < 2; ++hh)
#pragma unroll
            for (int sn = 0; sn < 2; ++sn) {
                const int r = lane & 31;
                const lds_t pa = r < 16 ? smem + F_KVA + (((hh * 2 + sn) * 2 + h) * 16 + r) * 16 : smem + F_ZA + ((hh * 2 + sn) * 2 + h) * 16;
                af[hh][sn] = as_frag(lds_r128(pa));
            }
        f32x16 d0 = mfma32(af[0][0], as_frag(qp[0]), zero16);
        f32x16 d1 = mfma32(af[1][0], as_frag(qp[1]), zero16);
        d0 = mfma32(af[0][1], as_frag(qm[0]), d0);
        d1 = mfma32(af[1][1], as_frag(qm[1]), d1);
        const float r0 = __builtin_amdgcn_rcpf(d0[8] + 1e-6f), r1 = __builtin_amdgcn_rcpf(d1[8] + 1e-6f);
        const unsigned t16 = (unsigned)(t0 + trow) >> 4;
        const unsigned ob = WS_O + (t16 * 16 + hp) * 1024 + (h * 16 + (lane & 15)) * 16;
        st_sc1(rs, ob, (u32x4){pk(opq(d0[0] * r0), opq(d0[1] * r0)), pk(opq(d0[2] * r0), opq(d0[3] * r0)),
                               pk(opq(d1[0] * r1), opq(d1[1] * r1)), pk(opq(d1[2] * r1), opq(d1[3] * r1))});
        st_sc1(rs, ob + 512, (u32x4){pk(opq(d0[4] * r0), opq(d0[5] * r0)), pk(opq(d0[6] * r0), opq(d0[7] * r0)),
                                     pk(opq(d1[4] * r1), opq(d1[5] * r1)), pk(opq(d1[6] * r1), opq(d1[7] * r1))});
    }
    WAIT_BAR(0);
    if (threadIdx.x == 0) set_flag(ws, WS_FO + rt * 128 + hp * 4, tag);
    const unsigned wooff = (unsigned)((8 * w + (lane >> 3)) * 1024 + (((lane & 7) ^ (((lane >> 4) & 3) << 1)) * 16));
#pragma unroll
    for (int K = 0; K < 16; ++K) glds16(wooff, (const char*)wo + n0 * 2 + (size_t)K * 32768, sm + F_WO + K * 4096 + w * 1024);
    unsigned us[4][2];
#pragma unroll
    for (int nt = 0; nt < 4; ++nt)
#pragma unroll
        for (int r2 = 0; r2 < 2; ++r2)
            us[nt][r2] = pk(opq(bflo(uu[nt][r2]) * bflo(dk[nt][r2])), opq(bfhi(uu[nt][r2]) * bfhi(dk[nt][r2])));
    if (w == 1) wait_flags(ws, WS_FY + ct * 128, 32, tag, lane);
    if (w == 2) wait_flags(ws, WS_FU + (rt64 >> 2) * 128 + (rt64 & 3) * 32, 8, tag, lane);
    if (w == 0) wait_flags(ws, WS_FO + (rt64 >> 1) * 128, 16, tag, lane);
    WAIT_BAR(0);
    u32x4 of[16];
    const unsigned ob = WS_O + (unsigned)(4 * rt64 + w) * 16384 + lane * 16;
#pragma unroll
    for (int k = 0; k < OFD; ++k) of[k] = ld_sc1(rs, ob + k * 1024);
    float g;
    auto fetch_y = [&]() {
#pragma unroll
        for (int ii = 0; ii < 2; ++ii) {
            const int dl = 16 * w + 8 * ii + (lane >> 3);
            glds16_sc1((unsigned)(dl * (SEQ * 2) + (((lane & 7) ^ (((dl >> 1) & 3) << 1)) * 16)), ws + WS_YT + ((size_t)n0 * SEQ + ft0) * 2,
                       sm + F_Y + (16 * w + 8 * ii) * 128);
        }
        g = __uint_as_float(__hip_atomic_load((const gu32*)(ws + WS_G + (t >> 3) * 128 + (t & 7) * 4), RLX_AGENT));
    };
    f32x4 fa4[4];
#pragma unroll
    for (int nt = 0; nt < 4; ++nt) fa4[nt] = (f32x4){0.f, 0.f, 0.f, 0.f};
    {
        lds_t wp[4];
#pragma unroll
        for (int nt = 0; nt < 4; ++nt) {
            const int k = 4 * kq + q;
            wp[nt] = opaque(smem + F_WO + k * 128 + (((2 * nt + (p >> 1)) ^ (((k >> 1) & 3) << 1)) * 16) + 8 * (p & 1));
        }
        bf16x8 wa2[2][4];
#pragma unroll
        for (int nt = 0; nt < 4; ++nt) wa2[0][nt] = frag2(lds_tr(wp[nt]), lds_tr(wp[nt] + 2048));
#pragma unroll
        for (int k = 0; k < 16; ++k) {
            SB();
            if (k + OFD < 16) of[k + OFD] = ld_sc1(rs, ob + (k + OFD) * 1024);
            if (k + OFD == 16) fetch_y();
            if (k + 1 < 16) {
#pragma unroll
                for (int nt = 0; nt < 4; ++nt) wa2[(k + 1) & 1][nt] = frag2(lds_tr(wp[nt] + (k + 1) * 4096), lds_tr(wp[nt] + (k + 1) * 4096 + 2048));
            }
            SB();
#pragma unroll
            for (int nt = 0; nt < 4; ++nt) fa4[nt] = mfma16(wa2[k & 1][nt], as_frag(of[k]), fa4[nt]);
        }
    }
    WAIT_BAR(0);
    u32x2 res[4];
    const float g1 = rbf(1.f - g);
#pragma unroll
    for (int nt = 0; nt < 4; ++nt) {
        const int dl = 16 * nt + 4 * kq + q;
        const int c = (16 * w + 4 * p) >> 3;
        const u32x2 yv = lds_tr(smem + F_Y + dl * 128 + ((c ^ (((dl >> 1) & 3) << 1)) * 16) + 8 * (p & 1));
        float o[4];
#pragma unroll
        for (int r2 = 0; r2 < 2; ++r2) {
            const unsigned a2 = pk(opq(bflo(yv[r2]) + bflo(us[nt][r2])), opq(bfhi(yv[r2]) + bfhi(us[nt][r2])));
            const unsigned b2 = pk(fa4[nt][2 * r2], fa4[nt][2 * r2 + 1]);
            const unsigned ga = pk(opq(g * bflo(a2)), opq(g * bfhi(a2)));
            const unsigned gb = pk(opq(g1 * bflo(b2)), opq(g1 * bfhi(b2)));
            o[2 * r2] = opq(bflo(ga) + bflo(gb));
            o[2 * r2 + 1] = opq(bfhi(ga) + bfhi(gb));
        }
        res[nt] = (u32x2){pk(o[0], o[1]), pk(o[2], o[3])};
    }
#pragma unroll
    for (int pr = 0; pr < 2; ++pr) {
        auto s0 = __builtin_amdgcn_permlane16_swap(res[2 * pr].x, res[2 * pr + 1].x, false, false);
        auto s1 = __builtin_amdgcn_permlane16_swap(res[2 * pr].y, res[2 * pr + 1].y, false, false);
        const int col = n0 + 32 * pr + (kq & 1) * 16 + (kq >> 1) * 8;
        *(u32x4*)(out + (size_t)t * DM + col) = (u32x4){s0[0], s1[0], s0[1], s1[1]};
    }
}

constexpr int CV_XY = 0;
constexpr int CV_EX = 0;
constexpr int CV_KR = 8192;
constexpr int CV_Q  = 17408;
constexpr int CV_E2 = 0;
constexpr int CV_UT = 69632;
constexpr int CV_SINK = 66304;
constexpr int QS = 4160;

DI f32x2 cexp_p(float a, float b, float p) {
    const float m = __builtin_amdgcn_exp2f(a * p * LOG2E);
    const float th = b * p;
    const float hi = th * 0.15915494309189535f;
    const float lo = __builtin_fmaf(th, 0.15915494309189535f, -hi) + th * 6.4206382149e-9f;
    const float fr = __builtin_amdgcn_fractf(hi) + lo;
    f32x2 r = {opq(m * __builtin_amdgcn_cosf(fr)), opq(m * __builtin_amdgcn_sinf(fr))};
    return r;
}
DI float exp_acc(float x) {
    const float hi = x * LOG2E;
    const float lo = __builtin_fmaf(x, LOG2E, -hi) + x * 1.925963033500011e-8f;
    return __builtin_amdgcn_exp2f(hi) * __builtin_fmaf(lo, 0.6931471805599453f, 1.f);
}
struct Cx { float x, y; };
DI Cx cmul(Cx a, Cx w) {
    Cx r;
    r.x = opq(__builtin_fmaf(-a.y, w.y, opq(a.x * w.x)));
    r.y = opq(__builtin_fmaf(a.y, w.x, opq(a.x * w.y)));
    return r;
}

struct CFrag { bf16x8 a[4][2], b[4]; };

DI void c_role(const u16* __restrict__ x, const u16* __restrict__ logA, const u16* __restrict__ Aim, const u16* __restrict__ Cre,
               const u16* __restrict__ Cim, const u16* __restrict__ logdt, const u16* __restrict__ gw, char* __restrict__ ws, int b, lds_t smem,
               const u16* __restrict__ wo, const u16* __restrict__ dskip) {
    const int tid = threadIdx.x, lane = tid & 63;
    const int w = __builtin_amdgcn_readfirstlane(tid >> 6);
    const int ch = w >> 1, hf = w & 1;
    const int cg = b & 7, ci_ = b >> 3;
    const int cb = 32 * cg + ci_;
    const int d = 2 * cb + ch;
    const rsrc_t rs = __builtin_amdgcn_make_buffer_rsrc((void*)ws, (short)0, (int)WS_END, 0x00020000);
    const unsigned tag = __hip_atomic_load((const gu32*)(ws + WS_E), RLX_AGENT) + 1u;

    u32x4 xt[2];
#pragma unroll
    for (int s = 0; s < 2; ++s) xt[s] = *(const u32x4*)(x + (size_t)(64 * ci_ + 32 * s + (tid >> 3)) * DM + 64 * cg + 8 * (tid & 7));
    const int n = 64 * hf + lane;
    const unsigned la = logA[d * 128 + n], ai = Aim[d * 128 + n], cr = Cre[d * 128 + n], ci = Cim[d * 128 + n], ld = logdt[d];
    u32x4 gx[2];
#pragma unroll
    for (int r = 0; r < 2; ++r) gx[r] = *(const u32x4*)(x + (size_t)(8 * cb + 2 * w + r) * DM + lane * 8);
    const u32x4 gwv = *(const u32x4*)(gw + lane * 8);
#pragma unroll
    for (int s = 0; s < 2; ++s) lds_w128(smem + CV_UT + (32 * s + (tid >> 3)) * 128 + (tid & 7) * 16, xt[s]);
    LDS_BAR();
    {
        const int g = lane >> 4, q = (lane & 15) >> 2, p = lane & 3;
#pragma unroll
        for (int s = 0; s < 2; ++s) {
            const lds_t a = smem + CV_UT + (8 * (g + 4 * s) + q) * 128 + 32 * w + 8 * p;
            const u32x2 lo = lds_tr(a), hi = lds_tr(a + 512);
            st_sc1(rs, WS_UX + (cg * 32 + ci_) * 8192 + (16 * w + (lane & 15)) * 128 + (g + 4 * s) * 16, (u32x4){lo.x, lo.y, hi.x, hi.y});
        }
    }
    {
        float gate[2];
#pragma unroll
        for (int r = 0; r < 2; ++r) {
            float s = 0.f;
#pragma unroll
            for (int j = 0; j < 4; ++j) {
                const unsigned pr = pk(opq(bflo(gx[r][j]) * bflo(gwv[j])), opq(bfhi(gx[r][j]) * bfhi(gwv[j])));
                s = opq(s + opq(bflo(pr) + bfhi(pr)));
            }
            s = sum_wave(s);
            gate[r] = rbf(__builtin_amdgcn_rcpf(1.f + __builtin_amdgcn_exp2f(-LOG2E * rbf(s))));
        }
        if (lane == 0)
            __hip_atomic_store((GAS unsigned long long*)(ws + WS_G + cb * 128 + w * 8),
                               ((unsigned long long)__float_as_uint(gate[1]) << 32) | __float_as_uint(gate[0]), RLX_AGENT);
    }

    const lds_t tw = smem + CV_XY + w * 16384;
    {
        const float dt = rbf(exp_acc(bf2f(ld)));
        const float a = -rbf(exp_acc(bf2f(la))) * dt;
        const float bb = bf2f(ai) * dt;
        const f32x2 e1 = cexp_p(a, bb, 1.f), e8 = cexp_p(a, bb, 8.f), e256 = cexp_p(a, bb, 256.f);
        const lds_t row = tw + lane * 128;
        const int sw = lane & 7;
        auto put8 = [&](int tbl, int chunk, const Cx* v) {
            lds_w128(row + tbl * 8192 + ((chunk ^ sw) * 16), (u32x4){pk(v[0].x, v[1].x), pk(v[2].x, v[3].x), pk(v[4].x, v[5].x), pk(v[6].x, v[7].x)});
            lds_w128(row + tbl * 8192 + (((4 + chunk) ^ sw) * 16), (u32x4){pk(v[0].y, v[1].y), pk(v[2].y, v[3].y), pk(v[4].y, v[5].y), pk(v[6].y, v[7].y)});
        };
        {
            const Cx m8 = {e8.x, e8.y};
            Cx v[8];
            v[0] = (Cx){1.f, 0.f};
            v[1] = m8;
#pragma unroll
            for (int c = 2; c < 8; ++c) v[c] = cmul(v[c - 1], m8);
            put8(1, 0, v);
#pragma unroll
            for (int k = 1; k < 4; ++k) {
                v[0] = cmul(v[7], m8);
#pragma unroll
                for (int c = 1; c < 8; ++c) v[c] = cmul(v[c - 1], m8);
                put8(1, k, v);
            }
        }
        WAIT_BAR(0);
        if (tid == 0) set_flag(ws, WS_FU + cg * 128 + ci_ * 4, tag);
        if (w >= 2) touch_lines((const char*)wo + ci_ * 16384 + (w - 2) * 8192 + lane * 128, ldsaddr(smem) + CV_SINK + (w - 2) * 256);
        if (w == 1) touch_lines((const char*)dskip + (lane & 7) * 128, ldsaddr(smem) + CV_SINK + 512);
        {
            const Cx m1 = {e1.x, opq(-e1.y)}, m8 = {e8.x, opq(-e8.y)}, m256 = {e256.x, opq(-e256.y)};
            const Cx m2 = cmul(m1, m1);
            const Cx m4 = cmul(m2, m2);
            Cx v[8];
            v[0] = (Cx){opq(2.f * bf2f(cr) * dt), opq(-2.f * bf2f(ci) * dt)};
            v[1] = cmul(v[0], m1); v[2] = cmul(v[1], m1); v[3] = cmul(v[2], m1);
            v[4] = cmul(v[0], m256);
            v[5] = cmul(v[4], m2); v[6] = cmul(v[5], m2); v[7] = cmul(v[6], m2);
            put8(0, 0, v);
            Cx b256 = v[4];
            v[0] = cmul(v[3], m1); v[1] = cmul(v[0], m1); v[2] = cmul(v[1], m1); v[3] = cmul(v[2], m1);
            v[4] = cmul(v[7], m2);
            v[5] = cmul(b256, m256);
            v[6] = cmul(v[5], m4); v[7] = cmul(v[6], m4);
            put8(0, 1, v);
            v[4] = cmul(v[5], m256);
            v[5] = cmul(v[4], m4); v[6] = cmul(v[5], m4);
            v[7] = (Cx){0.f, 0.f};
            v[0] = cmul(v[4], m256); v[1] = cmul(v[0], m8);
            v[2] = cmul(v[0], m256); v[3] = cmul(v[2], m8);
            put8(0, 2, v);
            v[0] = cmul(v[2], m256); v[1] = cmul(v[0], m8);
            v[2] = cmul(v[0], m256); v[3] = cmul(v[2], m8);
            v[4] = v[5] = v[6] = v[7];
            put8(0, 3, v);
        }
    }
    const int h = lane >> 5;
    f32x16 kd;
#pragma unroll
    for (int r = 0; r < 16; ++r) kd[r] = 0.f;
    {
        asm volatile("s_waitcnt lgkmcnt(0)" ::: "memory");
        const int blk = (lane >> 4) & 1, q = (lane & 15) >> 2, p = lane & 3;
        lds_t ra[2];
#pragma unroll
        for (int rho = 0; rho < 2; ++rho) {
            const int m = 4 * h + 2 * rho + (q >> 1);
            ra[rho] = opaque(tw + m * 128 + (((4 * (q & 1) + 2 * blk + (p >> 1)) ^ m) * 16) + 8 * (p & 1));
        }
#pragma unroll
        for (int s2 = 0; s2 < 2; ++s2) {
            bf16x8 fa[4], fb[4];
#pragma unroll
            for (int s = 0; s < 4; ++s) {
                fa[s] = frag2(lds_tr(ra[0] + (4 * s2 + s) * 1024), lds_tr(ra[1] + (4 * s2 + s) * 1024));
                fb[s] = frag2(lds_tr(ra[0] + 8192 + (4 * s2 + s) * 1024), lds_tr(ra[1] + 8192 + (4 * s2 + s) * 1024));
            }
#pragma unroll
            for (int s = 0; s < 4; ++s) kd = mfma32(fa[s], fb[s], kd);
        }
    }
    if (w == 0) wait_flags(ws, WS_FU + cg * 128, 32, tag, lane);
    LDS_BAR();
    u32x4 ux[2];
#pragma unroll
    for (int s = 0; s < 2; ++s) ux[s] = ld_sc1(rs, WS_UX + (cg * 32 + 16 * s + (tid >> 4)) * 8192 + ci_ * 256 + (tid & 15) * 16);
    {
        const lds_t e = smem + CV_EX + w * 2048 + lane * 16;
        f32x4 s0, s1;
        if (hf == 0) { s0 = (f32x4){kd[8], kd[9], kd[10], kd[11]}; s1 = (f32x4){kd[12], kd[13], kd[14], kd[15]}; }
        else         { s0 = (f32x4){kd[0], kd[1], kd[2], kd[3]};   s1 = (f32x4){kd[4], kd[5], kd[6], kd[7]}; }
        lds_w128(e, __builtin_bit_cast(u32x4, s0));
        lds_w128(e + 1024, __builtin_bit_cast(u32x4, s1));
    }
    LDS_BAR();
    {
        const lds_t e = smem + CV_EX + (w ^ 1) * 2048 + lane * 16;
        const f32x4 s0 = __builtin_bit_cast(f32x4, lds_r128(e)), s1 = __builtin_bit_cast(f32x4, lds_r128(e + 1024));
        float k[8];
#pragma unroll
        for (int r = 0; r < 4; ++r) { k[r] = opq((hf == 0 ? kd[r] : kd[8 + r]) + s0[r]); k[4 + r] = opq((hf == 0 ? kd[4 + r] : kd[12 + r]) + s1[r]); }
        const int c = lane & 31;
        const lds_t kr = smem + CV_KR + ch * 4352 + 16;
        if (hf == 0) {
            u32x4 v;
            if (h == 0) v = (u32x4){pk(k[7], k[6]), pk(k[5], k[4]), pk(k[3], k[2]), pk(k[1], k[0])};
            else        v = (u32x4){pk(k[4], k[3]), pk(k[2], k[2]), pk(k[2], k[1]), pk(k[0], k[0])};
            lds_w128(kr + (32 * h + c) * 16, v);
            if (h == 1) { v = (u32x4){pk(k[7], k[7]), pk(k[6], k[6]), pk(k[6], k[5]), pk(k[5], k[5])}; lds_w128(kr + (64 + c) * 16, v); }
            if (lane == 0) { const u32x4 z = {0u, 0u, 0u, 0u}; lds_w128(kr - 16, z); }
        } else {
            if (h == 1) {
                const u32x4 v = {pk(k[2], k[2]), pk(k[1], k[1]), pk(k[1], k[0]), pk(k[0], k[0])};
                lds_w128(kr + (96 + c) * 16, v);
            } else {
#pragma unroll
                for (int al = 0; al < 4; ++al) {
                    const float k0 = k[2 * al], k8 = k[2 * al + 1];
                    const u32x4 v = {pk(k8, k8), pk(k8, (c & 1) ? k8 : k0), pk(k0, k0), pk(k0, k0)};
                    lds_w128(kr + (128 + 32 * al + c) * 16, v);
                }
            }
        }
    }
    LDS_BAR();
    {
        const lds_t qb = smem + CV_Q + ch * 16640;
#pragma unroll
        for (int it = 0; it < 2; ++it) {
            const int c8 = 128 * hf + 64 * it + lane;
            const lds_t kr = smem + CV_KR + ch * 4352 + c8 * 16;
            const u32x4 r = lds_r128(kr + 16);
            const u32x2 pv = *(LAS u32x2*)kr;
            const unsigned a0 = __builtin_amdgcn_alignbit(r[1], r[0], 16), a1 = __builtin_amdgcn_alignbit(r[2], r[1], 16),
                           a2 = __builtin_amdgcn_alignbit(r[3], r[2], 16), a3 = __builtin_amdgcn_alignbit(pv[0], r[3], 16),
                           a4 = __builtin_amdgcn_alignbit(pv[1], pv[0], 16);
            const lds_t q = qb + (2 * c8 + 8) * 8;
            lds_w128(q,          (u32x4){a3, a4, a1, a2});
            lds_w128(q + QS,     (u32x4){r[3], pv[0], r[1], r[2]});
            lds_w128(q + 2 * QS, (u32x4){a2, a3, a0, a1});
            lds_w128(q + 3 * QS, (u32x4){r[2], r[3], r[0], r[1]});
        }
        const u32x4 z = {0u, 0u, 0u, 0u};
        if (hf == 0 && lane < 16) lds_w128(qb + (lane >> 2) * QS + (lane & 3) * 16, z);
        if (hf == 1) lds_w128(smem + CV_UT + ch * 5120 + (lane >> 4) * 1280 + (lane & 15) * 16, z);
#pragma unroll
        for (int s = 0; s < 2; ++s) {
            const int rtile = 16 * s + (tid >> 4), piece = tid & 15, chunk = piece & 7;
            lds_w128(smem + CV_UT + (piece >> 3) * 5120 + (chunk & 3) * 1280 + (2 * rtile + (chunk >> 2) + 16) * 16, ux[s]);
        }
    }
    LDS_BAR();
    f32x4 acc[4][2];
#pragma unroll
    for (int nn = 0; nn < 4; ++nn)
#pragma unroll
        for (int mb = 0; mb < 2; ++mb) acc[nn][mb] = (f32x4){0.f, 0.f, 0.f, 0.f};
    {
        const int r = lane & 15, kq = lane >> 4;
        const lds_t la0 = opaque(smem + CV_Q + ch * 16640 + (r & 3) * QS + ((r >> 2) - 2 * kq + 7) * 8 + hf * 8 * 64);
        const lds_t lb0 = opaque(smem + CV_UT + ch * 5120 + kq * 1280 + (r + 16) * 16 - hf * 8 * 16 - 7 * 16);
        auto rd_a = [&](CFrag& f, int dp, int m, int mb) {
            const lds_t p = la0 + dp * 64 + m * 1024 + mb * 32;
            const u32x2 f0 = lds_r64v(p + 8), f1 = lds_r64v(p);
            f.a[m][mb] = frag2(f0, f1);
        };
        auto rd_b = [&](CFrag& f, int dp, int mu) { f.b[mu] = as_frag(lds_r128(lb0 + (7 - dp) * 16 + mu * 256)); };
        CFrag fa, fb;
#pragma unroll
        for (int mu = 0; mu < 4; ++mu) rd_b(fa, 0, mu);
#pragma unroll
        for (int m = 0; m < 4; ++m) { rd_a(fa, 0, m, 0); rd_a(fa, 0, m, 1); }
        SB();
#define C_MM(cur, m, nn, mb) acc[nn][mb] = mfma16(cur.a[m][mb], cur.b[(nn) - (m)], acc[nn][mb])
#define C_STEP(cur, nxt, dp, HAVE)                                                                     \
        do {                                                                                           \
            if (HAVE) { rd_b(nxt, dp, 0); rd_b(nxt, dp, 1); }                                          \
            SB(); C_MM(cur, 0, 0, 0); C_MM(cur, 0, 0, 1); SB();                                        \
            if (HAVE) { rd_b(nxt, dp, 2); rd_b(nxt, dp, 3); }                                          \
            SB(); C_MM(cur, 0, 1, 0); C_MM(cur, 0, 1, 1); SB();                                        \
            if (HAVE) rd_a(nxt, dp, 0, 0);                                                             \
            SB(); C_MM(cur, 0, 2, 0); C_MM(cur, 0, 2, 1); SB();                                        \
            if (HAVE) rd_a(nxt, dp, 0, 1);                                                             \
            SB(); C_MM(cur, 0, 3, 0); C_MM(cur, 0, 3, 1); SB();                                        \
            if (HAVE) rd_a(nxt, dp, 1, 0);                                                             \
            SB(); C_MM(cur, 1, 1, 0); C_MM(cur, 1, 1, 1); SB();                                        \
            if (HAVE) rd_a(nxt, dp, 1, 1);                                                             \
            SB(); C_MM(cur, 1, 2, 0); C_MM(cur, 1, 2, 1); SB();                                        \
            if (HAVE) rd_a(nxt, dp, 2, 0);                                                             \
            SB(); C_MM(cur, 1, 3, 0); C_MM(cur, 1, 3, 1); SB();                                        \
            if (HAVE) rd_a(nxt, dp, 2, 1);                                                             \
            SB(); C_MM(cur, 2, 2, 0); C_MM(cur, 2, 2, 1); SB();                                        \
            if (HAVE) rd_a(nxt, dp, 3, 0);                                                             \
            SB(); C_MM(cur, 2, 3, 0); C_MM(cur, 2, 3, 1); SB();                                        \
            if (HAVE) rd_a(nxt, dp, 3, 1);                                                             \
            SB(); C_MM(cur, 3, 3, 0); C_MM(cur, 3, 3, 1); SB();                                        \
        } while (0)
#pragma unroll
        for (int dp = 0; dp < 8; dp += 2) {
            C_STEP(fa, fb, dp + 1, true);
            C_STEP(fb, fa, dp + 2, dp + 2 < 8);
        }
#undef C_STEP
#undef C_MM
    }
    {
        f32x4 mine[2][2], other[2][2];
        if (hf == 0) {
#pragma unroll
            for (int q = 0; q < 2; ++q)
#pragma unroll
                for (int mb = 0; mb < 2; ++mb) { mine[q][mb] = acc[q][mb]; other[q][mb] = acc[2 + q][mb]; }
        } else {
#pragma unroll
            for (int q = 0; q < 2; ++q)
#pragma unroll
                for (int mb = 0; mb < 2; ++mb) { mine[q][mb] = acc[2 + q][mb]; other[q][mb] = acc[q][mb]; }
        }
        const lds_t e = smem + CV_E2 + w * 4096 + lane * 16;
#pragma unroll
        for (int q = 0; q < 2; ++q)
#pragma unroll
            for (int mb = 0; mb < 2; ++mb) lds_w128(e + (2 * q + mb) * 1024, __builtin_bit_cast(u32x4, other[q][mb]));
        LDS_BAR();
        const lds_t e2 = smem + CV_E2 + (w ^ 1) * 4096 + lane * 16;
        const int kq = lane >> 4;
#pragma unroll
        for (int q = 0; q < 2; ++q) {
            u32x2 pm[2];
#pragma unroll
            for (int mb = 0; mb < 2; ++mb) {
                const f32x4 o = __builtin_bit_cast(f32x4, lds_r128(e2 + (2 * q + mb) * 1024));
                const f32x4 m4 = mine[q][mb];
                pm[mb] = (u32x2){pk(opq(m4[0] + o[0]), opq(m4[1] + o[1])), pk(opq(m4[2] + o[2]), opq(m4[3] + o[3]))};
            }
            auto s0 = __builtin_amdgcn_permlane16_swap(pm[0].x, pm[1].x, false, false);
            auto s1 = __builtin_amdgcn_permlane16_swap(pm[0].y, pm[1].y, false, false);
            const int t = 512 * (2 * hf + q) + 32 * (lane & 15) + (kq & 1) * 16 + (kq >> 1) * 8;
            st_sc1(rs, WS_YT + (unsigned)(d * SEQ + t) * 2, (u32x4){s0[0], s1[0], s0[1], s1[1]});
        }
    }
    WAIT_BAR(0);
    if (tid == 0) set_flag(ws, WS_FY + cg * 128 + ci_ * 4, tag);
    if (b == 0 && w == 0) {
#pragma nounroll
        for (unsigned spins = 0; spins < SPIN_CAP; ++spins) {
            asm volatile("" ::: "memory");
            const u32x4 fu = ld_sc1(rs, WS_FU + lane * 16);
            const u32x4 fk = ld_sc1(rs, WS_FKV + (lane >> 2) * 128 + (lane & 3) * 16);
            const bool ok = fu.x == tag && fu.y == tag && fu.z == tag && fu.w == tag && fk.x == tag && fk.y == tag && fk.z == tag && fk.w == tag;
            if (__all(ok)) break;
            __builtin_amdgcn_s_sleep(1);
        }
        if (lane == 0) set_flag(ws, WS_E, tag);
    }
}

__global__ __launch_bounds__(256, 2) void fused_mix(const u16* __restrict__ x, const u16* __restrict__ logA, const u16* __restrict__ Aim,
                                                    const u16* __restrict__ Cre, const u16* __restrict__ Cim, const u16* __restrict__ logdt,
                                                    const u16* __restrict__ dskip, const u16* __restrict__ wqkv, const u16* __restrict__ wphi,
                                                    const u16* __restrict__ wo, const u16* __restrict__ gw, char* __restrict__ ws,
                                                    u16* __restrict__ out) {
    __shared__ __attribute__((aligned(16))) char smem_[79872];
    const lds_t smem = (lds_t)smem_;
    const int b = blockIdx.x;
    if (b < 256) c_role(x, logA, Aim, Cre, Cim, logdt, gw, ws, b, smem, wo, dskip);
    else g_role(x, wqkv, wphi, wo, dskip, ws, out, b - 256, smem);
}

extern "C" void kernel_launch(void* const* d_in, const int* in_sizes, int n_in, void* d_out, int out_size, void* d_ws, size_t ws_size,
                              hipStream_t stream) {
    fused_mix<<<512, 256, 0, stream>>>((const u16*)d_in[0], (const u16*)d_in[1], (const u16*)d_in[2], (const u16*)d_in[3], (const u16*)d_in[4],
                                       (const u16*)d_in[5], (const u16*)d_in[6], (const u16*)d_in[8], (const u16*)d_in[9], (const u16*)d_in[10],
                                       (const u16*)d_in[11], (char*)d_ws, (u16*)d_out);
}
```
